```python
import torch

def _softmax(x, axis=-1):
    return torch.softmax(x, dim=axis)
def _gelu(x):
    return torch.nn.functional.gelu(x, approximate="tanh")
def _silu(x):
    return torch.nn.functional.silu(x)
def _sigmoid(x):
    return torch.sigmoid(x)
def _layernorm(x, axis=-1, eps=1e-5):
    m = x.mean(dim=axis, keepdim=True)
    v = x.var(dim=axis, keepdim=True, unbiased=False)
    return (x - m) / torch.sqrt(v + eps)
def _scatter_add(tgt, idx, src, axis=0):
    return tgt.clone().index_add_(axis, idx, src)
def _segment_sum(src, seg, n):
    out = torch.zeros((n,) + src.shape[1:], dtype=src.dtype)
    return out.index_add_(0, seg, src)
def _pad(x, pw):
    flat = tuple(v for pair in reversed(pw) for v in pair)
    return torch.nn.functional.pad(x, flat)

kernel_name = 'standalone__edge_featured_mpnn_egc__graph'

def setup_inputs():

    _w = (lambda a: (lambda t: t.to(torch.bfloat16) if t.is_floating_point() else t)(torch.as_tensor(a)))
    N, deg, D, De = 12500, 32, 128, max(16, 1)
    E = N * deg
    return {
        "x": _w((lambda *s: torch.randn(*s, dtype=torch.bfloat16) * 0.1)(N, D)),
        "e_attr": _w((lambda *s: torch.randn(*s, dtype=torch.bfloat16) * 0.1)(E, De)),
        "src": _w((lambda lo, hi, *s: torch.randint(lo, hi, s))(0, N, E).to(torch.int64)),
        "dst": _w((lambda lo, hi, *s: torch.randint(lo, hi, s))(0, N, E).to(torch.int64)),
        "u_w": _w((lambda *s: torch.randn(*s, dtype=torch.bfloat16) * 0.05)(D, D)),
        "v_w": _w((lambda *s: torch.randn(*s, dtype=torch.bfloat16) * 0.05)(D, D)),
        "g_w": _w((lambda *s: torch.randn(*s, dtype=torch.bfloat16) * 0.1)(2 * D + De, D)),
    }

def reference(x, e_attr, src, dst, u_w, v_w, g_w):
    N, D = x.shape
    g_in = torch.concatenate([x[dst], x[src], e_attr], axis=-1)
    eta = _sigmoid(g_in @ g_w)
    hv = x @ v_w
    msg = eta * hv[src]
    agg = _segment_sum(msg, dst, N)
    return _silu(x @ u_w + agg)
```

```cpp
#include <hip/hip_runtime.h>
#include <hip/hip_bf16.h>
#include <stdint.h>

namespace {

constexpr int N_NODES = 12500, N_EDGES = 400000, DM = 128;
constexpr int NPB = 14;
constexpr int BPG = 7;
constexpr int GN = NPB * BPG;
constexpr int NG = 128;
constexpr int NBK = NG * BPG;
constexpr int NP = 256;
constexpr int CH = 1568;
constexpr int SLOT_REC = 30;
constexpr int BCAP = 1024;
constexpr int NW = 12;
constexpr int NT2 = NW * 64;
constexpr float NLOG2E = -1.4426950408889634f;

constexpr size_t WS_G = 0;
constexpr size_t WS_G_HALF = (size_t)N_NODES * 256;
constexpr size_t WS_SLOTS = 6400000 + 1024 - (6400000 % 1024);
constexpr size_t WS_OVF = WS_SLOTS + (size_t)NP * NG * 128;
constexpr size_t WS_WF = WS_OVF + (size_t)NP * CH * 4;

typedef short bf16x8 __attribute__((ext_vector_type(8)));
typedef short s16x4 __attribute__((ext_vector_type(4)));
typedef float f32x16 __attribute__((ext_vector_type(16)));
typedef float f32x2 __attribute__((ext_vector_type(2)));
typedef __bf16 bf16x2_t __attribute__((ext_vector_type(2)));
typedef unsigned u32x4 __attribute__((ext_vector_type(4)));
typedef unsigned u32x2 __attribute__((ext_vector_type(2)));

#define MFMA32(a, b, c) __builtin_amdgcn_mfma_f32_32x32x16_bf16((a), (b), (c), 0, 0, 0)

__device__ __forceinline__ unsigned pack_bf16(float lo, float hi) {
  f32x2 v; v.x = lo; v.y = hi;
  bf16x2_t r = __builtin_convertvector(v, bf16x2_t);
  return __builtin_bit_cast(unsigned, r);
}
__device__ __forceinline__ float bf16_round(float x) {
  return __uint_as_float(pack_bf16(x, 0.f) << 16);
}
__device__ __forceinline__ int crow(int reg, int h) { return (reg & 3) + 8 * (reg >> 2) + 4 * h; }

__device__ __forceinline__ int wave_incl_scan(int x) {
  x += __builtin_amdgcn_update_dpp(0, x, 0x111, 0xf, 0xf, false);
  x += __builtin_amdgcn_update_dpp(0, x, 0x112, 0xf, 0xf, false);
  x += __builtin_amdgcn_update_dpp(0, x, 0x114, 0xf, 0xf, false);
  x += __builtin_amdgcn_update_dpp(0, x, 0x118, 0xf, 0xf, false);
  x += __builtin_amdgcn_update_dpp(0, x, 0x142, 0xa, 0xf, false);
  x += __builtin_amdgcn_update_dpp(0, x, 0x143, 0xc, 0xf, false);
  return x;
}

constexpr int GEMM_BLOCKS = 224;
constexpr int SORT_BLOCKS = NP;
constexpr int WPREP_BLOCKS = 8;

struct SortLds {
  unsigned cntb[NBK];
  unsigned startb[NBK + 8];
  unsigned stage[CH];
  unsigned img[NG * 32];
};
struct GemmLds {
  unsigned short w[2][128][32];
};
union PrepLds { SortLds s; GemmLds g; };

__global__ __launch_bounds__(512) void k_prep(const unsigned short* __restrict__ x, const int* __restrict__ src,
                                              const int* __restrict__ dst, const unsigned short* __restrict__ u_w,
                                              const unsigned short* __restrict__ v_w, const unsigned short* __restrict__ g_w,
                                              unsigned* __restrict__ G, unsigned* __restrict__ slots,
                                              unsigned* __restrict__ ovf, u32x4* __restrict__ wfrag) {
  __shared__ __attribute__((aligned(16))) PrepLds lds;
  const int tid = threadIdx.x, lane = tid & 63, wv = tid >> 6;
  const int blk = blockIdx.x;
  if (blk < GEMM_BLOCKS) {
    const int j = blk >> 3;
    const int rg = (blk & 7) + 8 * (j >> 2), cb = j & 3;
    if (rg * 256 >= N_NODES) return;
    const int m = lane & 31, h = lane >> 5;
    {
      const int row = tid >> 2, c16 = tid & 3;
      const u32x4 wb = *(const u32x4*)(g_w + (size_t)(128 + row) * DM + cb * 32 + c16 * 8);
      const u32x4 wvv = *(const u32x4*)(v_w + (size_t)row * DM + cb * 32 + c16 * 8);
      *(u32x4*)&lds.g.w[0][row][c16 * 8] = wb;
      *(u32x4*)&lds.g.w[1][row][c16 * 8] = wvv;
    }
    const int row0 = rg * 256 + wv * 32;
    int xr = row0 + m; xr = xr < N_NODES ? xr : N_NODES - 1;
    bf16x8 xa[8];
#pragma unroll
    for (int s = 0; s < 8; ++s) xa[s] = *(const bf16x8*)(x + (size_t)xr * DM + 16 * s + 8 * h);
    __syncthreads();
    if (row0 >= N_NODES) return;
    f32x16 accB, accV;
#pragma unroll
    for (int i = 0; i < 16; ++i) { accB[i] = 0.f; accV[i] = 0.f; }
    const int q = (lane & 15) >> 2, p = lane & 3, blk16 = (lane >> 4) & 1;
    typedef __attribute__((address_space(3))) s16x4* lds_s16x4_ptr;
#pragma unroll
    for (int s = 0; s < 8; ++s) {
      const int kr = 16 * s + 8 * h + q;
      s16x4 b0 = __builtin_amdgcn_ds_read_tr16_b64_v4i16((lds_s16x4_ptr)&lds.g.w[0][kr][blk16 * 16 + p * 4]);
      s16x4 b1 = __builtin_amdgcn_ds_read_tr16_b64_v4i16((lds_s16x4_ptr)&lds.g.w[0][kr + 4][blk16 * 16 + p * 4]);
      s16x4 v0 = __builtin_amdgcn_ds_read_tr16_b64_v4i16((lds_s16x4_ptr)&lds.g.w[1][kr][blk16 * 16 + p * 4]);
      s16x4 v1 = __builtin_amdgcn_ds_read_tr16_b64_v4i16((lds_s16x4_ptr)&lds.g.w[1][kr + 4][blk16 * 16 + p * 4]);
      const bf16x8 fb = __builtin_shufflevector(b0, b1, 0, 1, 2, 3, 4, 5, 6, 7);
      const bf16x8 fv = __builtin_shufflevector(v0, v1, 0, 1, 2, 3, 4, 5, 6, 7);
      accB = MFMA32(xa[s], fb, accB);
      accV = MFMA32(xa[s], fv, accV);
    }
    unsigned* Gh = G + (size_t)(cb >> 1) * (WS_G_HALF / 4) + (cb & 1) * 32 + m;
#pragma unroll
    for (int r = 0; r < 16; ++r) {
      const int row = row0 + crow(r, h);
      const float hv = bf16_round(accV[r]);
      float ih = __builtin_amdgcn_rcpf(hv);
      ih = __builtin_amdgcn_fmed3f(ih, -3.3895313892515355e38f, 3.3895313892515355e38f);
      const unsigned L = pack_bf16(ih, 0.f) & 0xffffu;
      const unsigned u = __float_as_uint(accB[r] * NLOG2E);
      unsigned mg = u & 0x7fffffffu;
      mg = mg < 0x7f7e0000u ? mg : 0x7f7e0000u;
      const int t = (int)(mg + 0x8000u - L);
      const unsigned H = t < 0 ? 0u : ((unsigned)t & 0xffff0000u);
      if (row < N_NODES) Gh[(size_t)row * 64] = (u & 0x80000000u) | H | L;
    }
  } else if (blk < GEMM_BLOCKS + SORT_BLOCKS) {
    const int pidx = blk - GEMM_BLOCKS;
    const int e0 = pidx * CH + tid * 4;
    const bool have = (tid < CH / 4) && (e0 < N_EDGES);
    int4 dv = make_int4(0, 0, 0, 0), sv = make_int4(0, 0, 0, 0);
    if (have) { dv = *(const int4*)(dst + e0); sv = *(const int4*)(src + e0); }
    for (int i = tid; i < NBK; i += 512) lds.s.cntb[i] = 0;
    __syncthreads();
    unsigned rec[4], bk[4], rk[4];
    if (have) {
      const int d4[4] = {dv.x, dv.y, dv.z, dv.w}, s4[4] = {sv.x, sv.y, sv.z, sv.w};
#pragma unroll
      for (int k = 0; k < 4; ++k) {
        unsigned d = (unsigned)d4[k]; d = d < (unsigned)N_NODES ? d : (unsigned)(N_NODES - 1);
        unsigned s = (unsigned)s4[k]; s = s < (unsigned)N_NODES ? s : (unsigned)(N_NODES - 1);
        const unsigned b = d / NPB, dl = d - b * NPB;
        const unsigned g = b / BPG, bg = b - g * BPG;
        bk[k] = b;
        rec[k] = ((unsigned)(tid * 4 + k) << 21) | (s << 7) | (bg << 4) | dl;
        rk[k] = atomicAdd(&lds.s.cntb[b], 1u);
      }
    }
    __syncthreads();
    if (wv == 0) {
      unsigned c[14]; unsigned sum = 0;
#pragma unroll
      for (int i = 0; i < 14; ++i) { c[i] = lds.s.cntb[lane * 14 + i]; sum += c[i]; }
      unsigned run = (unsigned)wave_incl_scan((int)sum) - sum;
#pragma unroll
      for (int gg = 0; gg < 2; ++gg) {
        const unsigned gstart = run;
        unsigned long long hd = 0;
#pragma unroll
        for (int i = 0; i < 7; ++i) {
          lds.s.startb[lane * 14 + gg * 7 + i] = run;
          run += c[gg * 7 + i];
          unsigned sc = run - gstart; sc = sc < (unsigned)SLOT_REC ? sc : (unsigned)SLOT_REC;
          hd |= (unsigned long long)sc << (5 * i);
        }
        const unsigned total = run - gstart;
        hd |= (unsigned long long)(total > (unsigned)SLOT_REC ? 1u : 0u) << 35;
        hd |= (unsigned long long)total << 36;
        hd |= (unsigned long long)gstart << 47;
        lds.s.img[(lane * 2 + gg) * 32 + 0] = (unsigned)hd;
        lds.s.img[(lane * 2 + gg) * 32 + 1] = (unsigned)(hd >> 32);
      }
    }
    __syncthreads();
    unsigned st[4];
    if (have) {
#pragma unroll
      for (int k = 0; k < 4; ++k) { st[k] = lds.s.startb[bk[k]]; lds.s.stage[st[k] + rk[k]] = rec[k]; }
    }
    __syncthreads();
    if (have) {
#pragma unroll
      for (int k = 0; k < 4; ++k) {
        const unsigned n = lds.s.cntb[bk[k]];
        unsigned rank = 0;
        for (unsigned i = 0; i < n; ++i) rank += lds.s.stage[st[k] + i] < rec[k] ? 1u : 0u;
        const unsigned P = st[k] + rank;
        const unsigned g = bk[k] / BPG;
        const unsigned idx = P - lds.s.startb[g * BPG];
        if (idx < (unsigned)SLOT_REC) lds.s.img[g * 32 + 2 + idx] = rec[k];
        else ovf[(size_t)pidx * CH + P] = rec[k];
      }
    }
    __syncthreads();
    u32x4* so = (u32x4*)(slots + (size_t)pidx * NG * 32);
    const u32x4* si = (const u32x4*)lds.s.img;
    so[tid] = si[tid];
    so[tid + 512] = si[tid + 512];
  } else {
    const int idx = (blk - GEMM_BLOCKS - SORT_BLOCKS) * 512 + tid;
    const int l = idx & 63, s = (idx >> 6) & 7, cb = (idx >> 9) & 3, mat = idx >> 11;
    const unsigned short* W = mat ? u_w : g_w;
    const int c = l & 31, h = l >> 5;
    unsigned v[8];
#pragma unroll
    for (int j = 0; j < 8; ++j) v[j] = W[(size_t)(16 * s + 8 * h + j) * DM + cb * 32 + c];
    u32x4 o;
    o.x = v[0] | (v[1] << 16); o.y = v[2] | (v[3] << 16); o.z = v[4] | (v[5] << 16); o.w = v[6] | (v[7] << 16);
    wfrag[idx] = o;
  }
}

constexpr int SROW = 80;
constexpr int SMAT = 17 * SROW;
struct EdgeLds {
  unsigned rowoff[BPG * BCAP];
  unsigned eoffdl[BPG * BCAP];
  float agg[BPG * 2 * 8 * 64];
  u32x4 At[BPG * 2 * 64];
  u32x4 lut[17 * 2];
  unsigned char smat[NW * SMAT];
  u32x2 hdr[NP];
  short adj[NP * 8];
  unsigned tot[8];
  unsigned seg[8];
  unsigned long long omask[4];
  int finb[NW];
};

__device__ __forceinline__ int launder(int v) { asm volatile("" : "+v"(v)); return v; }

__device__ __forceinline__ void load_slots(const unsigned* __restrict__ slots, int grp, int tid, u32x4 (&pc)[3]) {
#pragma unroll
  for (int i = 0; i < 3; ++i) {
    const int q = tid + NT2 * i;
    pc[i] = u32x4{0, 0, 0, 0};
    if (q < NP * 8) pc[i] = *(const u32x4*)(slots + ((size_t)(q >> 3) * NG + grp) * 32 + (q & 7) * 4);
  }
}
__device__ __forceinline__ void place_slots(EdgeLds& lds, const u32x4 (&pc)[3], int tid, int win) {
#pragma unroll
  for (int i = 0; i < 3; ++i) {
    const int q = tid + NT2 * i;
    if (q < NP * 8) {
      const int p = q >> 3, part = q & 7;
      const int tp = lds.adj[p * 8 + 7];
      const unsigned r4[4] = {pc[i].x, pc[i].y, pc[i].z, pc[i].w};
#pragma unroll
      for (int k = 0; k < 4; ++k) {
        const int idx = part * 4 + k - 2;
        if (idx >= 0 && idx < tp) {
          const unsigned rec = r4[k];
          const int b = (rec >> 4) & 7;
          const int pos = lds.adj[p * 8 + b] + idx - win * BCAP;
          if ((unsigned)pos < (unsigned)BCAP) {
            lds.rowoff[b * BCAP + pos] = ((rec >> 7) & 0x3fffu) << 8;
            lds.eoffdl[b * BCAP + pos] = (((unsigned)p * CH + (rec >> 21)) << 5) | (rec & 15u);
          }
        }
      }
    }
  }
}

__global__ __launch_bounds__(NT2) void k_edge(const unsigned short* __restrict__ x, const unsigned short* __restrict__ e_attr,
                                              const unsigned short* __restrict__ g_w, const unsigned* __restrict__ G,
                                              const unsigned* __restrict__ slots, const unsigned* __restrict__ ovf,
                                              const u32x4* __restrict__ wfrag, unsigned short* __restrict__ out) {
  __shared__ __attribute__((aligned(16))) EdgeLds lds;
  const int tid = threadIdx.x, lane = tid & 63;
  const int wv = __builtin_amdgcn_readfirstlane(tid >> 6);
  const int blk = blockIdx.x;
  const int hf = (blk >> 2) & 1;
  const int grp = (blk >> 3) * 4 + (blk & 3);
  const int c = lane & 31, h = lane >> 5;

  u32x4 pc[3];
  load_slots(slots, grp, tid, pc);
  const int nunit = wv < 6 ? 1 : (wv == 6 ? 0 : 2);
  const int unit0 = wv < 6 ? wv : 6 + (wv - 7) * 2;

  if (tid < 34) {
    const int dl = tid >> 1, hh = tid & 1;
    u32x4 v = u32x4{0, 0, 0, 0};
    if (dl < 16 && ((dl >> 2) & 1) == hh) {
      const int j = ((dl >> 3) << 2) | (dl & 3);
      const unsigned one = (j & 1) ? 0x3f800000u : 0x00003f80u;
      v.x = (j >> 1) == 0 ? one : 0u; v.y = (j >> 1) == 1 ? one : 0u; v.z = (j >> 1) == 2 ? one : 0u; v.w = (j >> 1) == 3 ? one : 0u;
    }
    lds.lut[tid] = v;
  }
  for (int i = tid; i < NW * SMAT / 16; i += NT2) ((u32x4*)lds.smat)[i] = u32x4{0, 0, 0, 0};

  bf16x8 wch[2], wcl[2];
#pragma unroll
  for (int cb = 0; cb < 2; ++cb) {
    unsigned hi[4], lo[4];
#pragma unroll
    for (int jj = 0; jj < 4; ++jj) {
      const float w0 = __uint_as_float((unsigned)g_w[(size_t)(256 + 8 * h + 2 * jj) * DM + hf * 64 + cb * 32 + c] << 16) * NLOG2E;
      const float w1 = __uint_as_float((unsigned)g_w[(size_t)(256 + 8 * h + 2 * jj + 1) * DM + hf * 64 + cb * 32 + c] << 16) * NLOG2E;
      hi[jj] = pack_bf16(w0, w1);
      lo[jj] = pack_bf16(w0 - __uint_as_float(hi[jj] << 16), w1 - __uint_as_float(hi[jj] & 0xffff0000u));
    }
    wch[cb] = __builtin_bit_cast(bf16x8, u32x4{hi[0], hi[1], hi[2], hi[3]});
    wcl[cb] = __builtin_bit_cast(bf16x8, u32x4{lo[0], lo[1], lo[2], lo[3]});
  }

#pragma unroll
  for (int i = 0; i < 3; ++i) {
    const int q = tid + NT2 * i;
    if (q < NP * 8 && (q & 7) == 0) lds.hdr[q >> 3] = u32x2{pc[i].x, pc[i].y};
  }
  __syncthreads();
  if (wv < 8) {
    const u32x4 ha = *(const u32x4*)&lds.hdr[lane * 4];
    const u32x4 hb = *(const u32x4*)&lds.hdr[lane * 4 + 2];
    unsigned long long H[4];
    H[0] = ((unsigned long long)ha.y << 32) | ha.x; H[1] = ((unsigned long long)ha.w << 32) | ha.z;
    H[2] = ((unsigned long long)hb.y << 32) | hb.x; H[3] = ((unsigned long long)hb.w << 32) | hb.z;
    if (wv < 7) {
      int st[4], cn[4], sum = 0;
#pragma unroll
      for (int k = 0; k < 4; ++k) {
        const unsigned f = (unsigned)((H[k] << 5) >> (5 * wv));
        st[k] = f & 31; cn[k] = ((f >> 5) & 31) - st[k]; sum += cn[k];
      }
      const int inc = wave_incl_scan(sum);
      int run = inc - sum;
#pragma unroll
      for (int k = 0; k < 4; ++k) { lds.adj[(lane * 4 + k) * 8 + wv] = (short)(run - st[k]); run += cn[k]; }
      if (lane == 63) lds.tot[wv] = (unsigned)inc;
    } else {
#pragma unroll
      for (int k = 0; k < 4; ++k) {
        lds.adj[(lane * 4 + k) * 8 + 7] = (short)((H[k] >> 30) & 31);
      }
#pragma unroll
      for (int k = 0; k < 4; ++k) {
        const unsigned long long mk = __ballot((int)((H[k] >> 35) & 1));
        if (lane == 0) lds.omask[k] = mk;
      }
    }
  }
  for (int u = 0; u < nunit; ++u) {
    const int unit = unit0 + u, rb = unit >> 2, cu = unit & 3;
    const int bkt = 2 * rb + (c >> 4);
    int dl = c & 15; dl = dl < NPB ? dl : NPB - 1;
    int node = grp * GN + (bkt < BPG ? bkt : BPG - 1) * NPB + dl; node = node < N_NODES ? node : N_NODES - 1;
    const u32x4* wf = wfrag + ((size_t)((cu >> 1) * 4 + hf * 2 + (cu & 1)) * 8) * 64 + lane;
    f32x16 acc;
#pragma unroll
    for (int i = 0; i < 16; ++i) acc[i] = 0.f;
    bf16x8 xa[8], wb[8];
#pragma unroll
    for (int s = 0; s < 8; ++s) {
      xa[s] = *(const bf16x8*)(x + (size_t)node * DM + 16 * s + 8 * h);
      wb[s] = __builtin_bit_cast(bf16x8, wf[s * 64]);
    }
#pragma unroll
    for (int s = 0; s < 8; ++s) acc = MFMA32(xa[s], wb[s], acc);
    const int cb = cu & 1;
#pragma unroll
    for (int hb = 0; hb < 2; ++hb) {
      const int b = 2 * rb + hb;
      if (b < BPG) {
        if (cu < 2) {
          u32x4 pk;
          pk.x = pack_bf16(acc[8 * hb + 0] * NLOG2E, acc[8 * hb + 1] * NLOG2E);
          pk.y = pack_bf16(acc[8 * hb + 2] * NLOG2E, acc[8 * hb + 3] * NLOG2E);
          pk.z = pack_bf16(acc[8 * hb + 4] * NLOG2E, acc[8 * hb + 5] * NLOG2E);
          pk.w = pack_bf16(acc[8 * hb + 6] * NLOG2E, acc[8 * hb + 7] * NLOG2E);
          lds.At[(b * 2 + cb) * 64 + lane] = pk;
        } else {
#pragma unroll
          for (int r = 0; r < 8; ++r) lds.agg[((b * 2 + cb) * 8 + r) * 64 + lane] = acc[8 * hb + r];
        }
      }
    }
  }
  __syncthreads();

  const __amdgpu_buffer_rsrc_t rsG =
      __builtin_amdgcn_make_buffer_rsrc((void*)((const char*)G + (size_t)hf * WS_G_HALF), 0, (int)WS_G_HALF, 0x00020000);
  const __amdgpu_buffer_rsrc_t rsE =
      __builtin_amdgcn_make_buffer_rsrc((void*)e_attr, 0, N_EDGES * 32, 0x00020000);
  const unsigned laneoff = (unsigned)c * 4u;
  unsigned char* smat = lds.smat + wv * SMAT;
  const unsigned spos = (unsigned)((c >> 4) * 32 + ((c >> 2) & 1) * 16 + ((((c >> 3) & 1) << 2) | (c & 3)) * 2);
  const unsigned srd = (unsigned)((c & 15) * SROW + h * 16);

  int src_p = -1, win = 0;
  place_slots(lds, pc, tid, 0);
  for (;;) {
    int cum[BPG + 1]; int maxtot = 0;
    cum[0] = 0;
#pragma unroll
    for (int b = 0; b < BPG; ++b) {
      const int t = __builtin_amdgcn_readfirstlane((int)lds.tot[b]);
      maxtot = t > maxtot ? t : maxtot;
      int rc = t - win * BCAP; rc = rc < 0 ? 0 : (rc > BCAP ? BCAP : rc);
      cum[b + 1] = cum[b] + ((rc + 31) >> 5);
    }
    {
      const int tl = launder(tid);
      if (tl < BPG * 32) {
        const int b = tl >> 5;
        int rc = (int)lds.tot[b] - win * BCAP; rc = rc < 0 ? 0 : (rc > BCAP ? BCAP : rc);
        const int e = rc + (tl & 31);
        if (e < ((rc + 31) & ~31)) { lds.rowoff[b * BCAP + e] = 0u; lds.eoffdl[b * BCAP + e] = 16u; }
      }
    }
    __syncthreads();

    const int T = cum[BPG];
    const int t0 = __builtin_amdgcn_readfirstlane((wv * T) / NW), t1 = __builtin_amdgcn_readfirstlane(((wv + 1) * T) / NW);
    const int ntile = t1 - t0;
    f32x16 acc0, acc1;
#pragma unroll
    for (int i = 0; i < 16; ++i) { acc0[i] = 0.f; acc1[i] = 0.f; }
    int curb = -1;
    if (ntile > 0) {
      int vent, vend;
      {
        int tau = t0 + lane; tau = tau < t1 ? tau : t1 - 1;
        int b = 0, cb0 = 0, ce = cum[1];
#pragma unroll
        for (int k = 1; k < BPG; ++k) { if (tau >= cum[k]) { b = k; cb0 = cum[k]; ce = cum[k + 1]; } }
        vent = b * BCAP + (tau - cb0) * 32;
        vend = (ce < t1 ? ce : t1) - t0;
      }
      const u32x4* rowoff4 = (const u32x4*)lds.rowoff;
      int ent0 = __builtin_amdgcn_readlane(vent, 0);
      int ent1 = __builtin_amdgcn_readlane(vent, 1);
      u32x4 eaC = __builtin_amdgcn_raw_buffer_load_b128(rsE, (lds.eoffdl[ent0 + c] & ~31u) + h * 16, 0, 0);
      const unsigned recN = lds.eoffdl[ent1 + c];
      unsigned ad[16];
#pragma unroll
      for (int qd = 0; qd < 4; ++qd) {
        const u32x4 ro = rowoff4[(ent0 >> 2) + 2 * qd + h];
        ad[4 * qd + 0] = ro.x + laneoff; ad[4 * qd + 1] = ro.y + laneoff; ad[4 * qd + 2] = ro.z + laneoff; ad[4 * qd + 3] = ro.w + laneoff;
      }
      f32x16 G0, G1;
      __builtin_amdgcn_sched_barrier(0);
#pragma unroll
      for (int r = 0; r < 16; ++r) G0[r] = __uint_as_float(__builtin_amdgcn_raw_buffer_load_b32(rsG, ad[r], 0, 0));
      __builtin_amdgcn_sched_barrier(0);
      u32x4 eaN = __builtin_amdgcn_raw_buffer_load_b128(rsE, (recN & ~31u) + h * 16, 0, 0);
      __builtin_amdgcn_sched_barrier(0);
#pragma unroll
      for (int r = 0; r < 16; ++r) G1[r] = __uint_as_float(__builtin_amdgcn_raw_buffer_load_b32(rsG, ad[r] + 128, 0, 0));
      __builtin_amdgcn_sched_barrier(0);
      int it = 0;
      for (;;) {
        curb = ent0 >> 10;
        const int segend = __builtin_amdgcn_readlane(vend, it);
        const bf16x8 A0 = __builtin_bit_cast(bf16x8, lds.At[(curb * 2 + 0) * 64 + lane]);
        const bf16x8 A1 = __builtin_bit_cast(bf16x8, lds.At[(curb * 2 + 1) * 64 + lane]);
        do {
          const int ent2 = __builtin_amdgcn_readlane(vent, it + 2 < 63 ? it + 2 : 63);
          const unsigned dl = lds.eoffdl[ent0 + c] & 31u;
          const bf16x8 oh = __builtin_bit_cast(bf16x8, lds.lut[dl * 2 + h]);
          unsigned short* sp = (unsigned short*)(smat + dl * SROW + spos);
          *sp = (unsigned short)0x3f80;
          const bf16x8 S0 = *(const bf16x8*)(smat + srd);
          const bf16x8 S1 = *(const bf16x8*)(smat + srd + 32);
          *sp = (unsigned short)0;
#pragma unroll
          for (int qd = 0; qd < 4; ++qd) {
            const u32x4 ro = rowoff4[(ent1 >> 2) + 2 * qd + h];
            ad[4 * qd + 0] = ro.x + laneoff; ad[4 * qd + 1] = ro.y + laneoff; ad[4 * qd + 2] = ro.z + laneoff; ad[4 * qd + 3] = ro.w + laneoff;
          }
          const unsigned rec2 = lds.eoffdl[ent2 + c];
          const bf16x8 ea = __builtin_bit_cast(bf16x8, eaC);
          {
            float ih[16];
#pragma unroll
            for (int r = 0; r < 16; ++r) ih[r] = __uint_as_float(__float_as_uint(G0[r]) << 16);
            G0 = MFMA32(ea, wch[0], G0);
            G0 = MFMA32(ea, wcl[0], G0);
            G0 = MFMA32(oh, A0, G0);
            unsigned P[8];
#pragma unroll
            for (int r = 0; r < 16; r += 2) {
              const float e0 = __builtin_amdgcn_exp2f(G0[r]), e1 = __builtin_amdgcn_exp2f(G0[r + 1]);
              const float m0 = __builtin_amdgcn_rcpf(__builtin_fmaf(e0, ih[r], ih[r]));
              const float m1 = __builtin_amdgcn_rcpf(__builtin_fmaf(e1, ih[r + 1], ih[r + 1]));
              P[r >> 1] = pack_bf16(m0, m1);
            }
#pragma unroll
            for (int r = 0; r < 16; ++r) G0[r] = __uint_as_float(__builtin_amdgcn_raw_buffer_load_b32(rsG, ad[r], 0, 0));
            acc0 = MFMA32(S0, __builtin_bit_cast(bf16x8, u32x4{P[0], P[1], P[2], P[3]}), acc0);
            acc0 = MFMA32(S1, __builtin_bit_cast(bf16x8, u32x4{P[4], P[5], P[6], P[7]}), acc0);
            asm volatile("" : "+v"(acc0));
          }
          __builtin_amdgcn_sched_barrier(0);
          {
            float ih[16];
#pragma unroll
            for (int r = 0; r < 16; ++r) ih[r] = __uint_as_float(__float_as_uint(G1[r]) << 16);
            G1 = MFMA32(ea, wch[1], G1);
            G1 = MFMA32(ea, wcl[1], G1);
            G1 = MFMA32(oh, A1, G1);
            asm volatile("v_mov_b32 %0, %4\n\tv_mov_b32 %1, %5\n\tv_mov_b32 %2, %6\n\tv_mov_b32 %3, %7"
                         : "=&v"(eaC.x), "=&v"(eaC.y), "=&v"(eaC.z), "=&v"(eaC.w) : "v"(eaN.x), "v"(eaN.y), "v"(eaN.z), "v"(eaN.w));
            eaN = __builtin_amdgcn_raw_buffer_load_b128(rsE, (rec2 & ~31u) + h * 16, 0, 0);
            unsigned P[8];
#pragma unroll
            for (int r = 0; r < 16; r += 2) {
              const float e0 = __builtin_amdgcn_exp2f(G1[r]), e1 = __builtin_amdgcn_exp2f(G1[r + 1]);
              const float m0 = __builtin_amdgcn_rcpf(__builtin_fmaf(e0, ih[r], ih[r]));
              const float m1 = __builtin_amdgcn_rcpf(__builtin_fmaf(e1, ih[r + 1], ih[r + 1]));
              P[r >> 1] = pack_bf16(m0, m1);
            }
#pragma unroll
            for (int r = 0; r < 16; ++r) G1[r] = __uint_as_float(__builtin_amdgcn_raw_buffer_load_b32(rsG, ad[r] + 128, 0, 0));
            acc1 = MFMA32(S0, __builtin_bit_cast(bf16x8, u32x4{P[0], P[1], P[2], P[3]}), acc1);
            acc1 = MFMA32(S1, __builtin_bit_cast(bf16x8, u32x4{P[4], P[5], P[6], P[7]}), acc1);
            asm volatile("" : "+v"(acc1));
          }
          __builtin_amdgcn_sched_barrier(0);
          ent0 = ent1; ent1 = ent2;
          ++it;
        } while (it < segend);
        if (it >= ntile) break;
        float* ag = &lds.agg[(curb * 2) * 8 * 64 + (launder(tid) & 63)];
#pragma unroll
        for (int r = 0; r < 8; ++r) { ag[r * 64] += acc0[r]; ag[(8 + r) * 64] += acc1[r]; }
#pragma unroll
        for (int i = 0; i < 16; ++i) { acc0[i] = 0.f; acc1[i] = 0.f; }
      }
    }
    __syncthreads();
    const int tl = launder(tid);
    {
      float* fin = (float*)lds.rowoff + wv * 1024 + (tl & 63);
#pragma unroll
      for (int r = 0; r < 8; ++r) { fin[r * 64] = acc0[r]; fin[(8 + r) * 64] = acc1[r]; }
      if ((tl & 63) == 0) lds.finb[wv] = curb;
    }
    bool more = false;
    int nsrc = src_p, nwin = win + 1;
    if (maxtot > nwin * BCAP) {
      more = true;
    } else {
      nwin = 0;
      int best = NP;
#pragma unroll
      for (int k = 0; k < 4; ++k) {
        unsigned long long mk = lds.omask[k];
        const int lo = (src_p + 1 - k + 3) >> 2;
        mk = lo >= 64 ? 0ull : (mk >> lo) << lo;
        if (mk) { const int p = 4 * (int)__builtin_ctzll(mk) + k; best = p < best ? p : best; }
      }
      if (best < NP) { nsrc = best; more = true; }
    }
    nsrc = __builtin_amdgcn_readfirstlane(nsrc);
    more = __builtin_amdgcn_readfirstlane((int)more) != 0;
    __syncthreads();
    for (int i = tl; i < BPG * 1024; i += NT2) {
      const int b = i >> 10;
      float v = lds.agg[i];
#pragma unroll
      for (int w = 0; w < NW; ++w) if (lds.finb[w] == b) v += ((const float*)lds.rowoff)[w * 1024 + (i & 1023)];
      lds.agg[i] = v;
    }
    if (!more) break;
    const u32x2 hd = lds.hdr[nsrc < 0 ? 0 : nsrc];
    const unsigned long long H = ((unsigned long long)hd.y << 32) | hd.x;
    const unsigned seg_base = (unsigned)((H >> 47) & 2047u) + SLOT_REC;
    const unsigned seg_n = (unsigned)((H >> 36) & 2047u) - SLOT_REC;
    if (nsrc != src_p) {
      if (tl < 8) lds.tot[tl] = 0;
      __syncthreads();
      for (unsigned i = tl; i < seg_n; i += NT2) {
        const unsigned rec = ovf[(size_t)nsrc * CH + seg_base + i];
        atomicAdd(&lds.tot[(rec >> 4) & 7], 1u);
      }
      __syncthreads();
      if (tl < 8) { unsigned sg = 0; for (int b = 0; b < tl; ++b) sg += lds.tot[b]; lds.seg[tl] = sg; }
    }
    src_p = nsrc; win = nwin;
    __syncthreads();
    if (src_p < 0) {
      u32x4 pc2[3];
      load_slots(slots, grp, tl, pc2);
      place_slots(lds, pc2, tl, win);
    } else {
      for (unsigned i = tl; i < seg_n; i += NT2) {
        const unsigned rec = ovf[(size_t)src_p * CH + seg_base + i];
        const int b = (rec >> 4) & 7;
        const int pos = (int)i - (int)lds.seg[b] - win * BCAP;
        if ((unsigned)pos < (unsigned)BCAP) {
          lds.rowoff[b * BCAP + pos] = ((rec >> 7) & 0x3fffu) << 8;
          lds.eoffdl[b * BCAP + pos] = (((unsigned)src_p * CH + (rec >> 21)) << 5) | (rec & 15u);
        }
      }
    }
  }
  __syncthreads();
  for (int t = launder(tid); t < GN * 8; t += NT2) {
    const int ln = t >> 3, f8 = t & 7;
    const int node = grp * GN + ln;
    if (node < N_NODES) {
      const int b = ln / NPB, dl = ln - b * NPB;
      const int r = ((dl >> 3) << 2) | (dl & 3), hh = (dl >> 2) & 1;
      const float* ag = &lds.agg[((b * 2 + (f8 >> 2)) * 8 + r) * 64 + hh * 32 + (f8 & 3) * 8];
      float z[8];
      *(float4*)&z[0] = *(const float4*)ag;
      *(float4*)&z[4] = *(const float4*)(ag + 4);
      unsigned o[4];
#pragma unroll
      for (int k = 0; k < 4; ++k) {
        const float a = z[2 * k] * __builtin_amdgcn_rcpf(1.f + __builtin_amdgcn_exp2f(z[2 * k] * NLOG2E));
        const float bb = z[2 * k + 1] * __builtin_amdgcn_rcpf(1.f + __builtin_amdgcn_exp2f(z[2 * k + 1] * NLOG2E));
        o[k] = pack_bf16(a, bb);
      }
      *(u32x4*)(out + (size_t)node * DM + hf * 64 + f8 * 8) = u32x4{o[0], o[1], o[2], o[3]};
    }
  }
}

}

extern "C" void kernel_launch(void* const* d_in, const int* in_sizes, int n_in, void* d_out, int out_size,
                              void* d_ws, size_t ws_size, hipStream_t stream) {
  const unsigned short* x = (const unsigned short*)d_in[0];
  const unsigned short* e_attr = (const unsigned short*)d_in[1];
  const int* src = (const int*)d_in[2];
  const int* dst = (const int*)d_in[3];
  const unsigned short* u_w = (const unsigned short*)d_in[4];
  const unsigned short* v_w = (const unsigned short*)d_in[5];
  const unsigned short* g_w = (const unsigned short*)d_in[6];
  char* ws = (char*)d_ws;
  unsigned* G = (unsigned*)(ws + WS_G);
  unsigned* slots = (unsigned*)(ws + WS_SLOTS);
  unsigned* ovf = (unsigned*)(ws + WS_OVF);
  u32x4* wfrag = (u32x4*)(ws + WS_WF);
  k_prep<<<GEMM_BLOCKS + SORT_BLOCKS + WPREP_BLOCKS, 512, 0, stream>>>(x, src, dst, u_w, v_w, g_w, G, slots, ovf, wfrag);
  k_edge<<<256, NT2, 0, stream>>>(x, e_attr, g_w, G, slots, ovf, wfrag, (unsigned short*)d_out);
}
```

```cpp
#include <hip/hip_runtime.h>
#include <hip/hip_bf16.h>
#include <stdint.h>

namespace {

constexpr int N_NODES = 12500, N_EDGES = 400000, DM = 128;
constexpr int GN = 49;
constexpr int NG = 256;
constexpr int BPG = 6;
constexpr int NKEY = 12;
constexpr int SRC_HALF = 6250;
constexpr int NP = 256;
constexpr int CH = 1568;
constexpr int FIX = 8;
constexpr int SLOT_REC = 28;
constexpr int BCAP = 384;
constexpr int POOL = 384;
constexpr int NW = 12;
constexpr int NT2 = NW * 64;
constexpr float NLOG2E = -1.4426950408889634f;
constexpr unsigned OOB = 0x80000000u;

constexpr size_t TAB_BYTES = (size_t)N_NODES * 512;
constexpr size_t WS_G = 0;
constexpr size_t WS_AH = 6400000;
constexpr size_t WS_SL = 12800000;
constexpr size_t WS_EAF = WS_SL + (size_t)NP * NG * 64;
constexpr size_t WS_EAS = WS_EAF + (size_t)NP * NG * 256;
constexpr size_t WS_OVF = WS_EAS + (size_t)NP * CH * 32;
constexpr size_t WS_WF = WS_OVF + (size_t)NP * CH * 4 + 256;

typedef short bf16x8 __attribute__((ext_vector_type(8)));
typedef short s16x4 __attribute__((ext_vector_type(4)));
typedef float f32x16 __attribute__((ext_vector_type(16)));
typedef float f32x2 __attribute__((ext_vector_type(2)));
typedef __bf16 bf16x2_t __attribute__((ext_vector_type(2)));
typedef unsigned u32x4 __attribute__((ext_vector_type(4)));
typedef __attribute__((address_space(3))) s16x4* lds_s16x4_ptr;
typedef __attribute__((address_space(3))) void* lds_ptr;

#define STAMP(k, i) do {} while (0)
#define NSTAMP(n) 0
#define GLOAD(a) __uint_as_float(__builtin_amdgcn_raw_buffer_load_b32(rsG, (a), 0, 0))
#define EXP2(x) __builtin_amdgcn_exp2f(x)
#define RCP(x) __builtin_amdgcn_rcpf(x)
#define MFMA32(a, b, c) __builtin_amdgcn_mfma_f32_32x32x16_bf16((a), (b), (c), 0, 0, 0)
#define DMA16(rs, ldsp, vo, so)                                                                               \
  asm volatile("s_mov_b32 m0, %0\n\ts_nop 0\n\tbuffer_load_dwordx4 %1, %2, %3 offen lds"                   \
               :: "s"((unsigned)(uintptr_t)(lds_ptr)(ldsp)), "v"((unsigned)(vo)), "s"(rs), "s"((unsigned)(so)) : "memory")
#define DMA16R(rs, ldsa, vo, so)                                                                              \
  asm volatile("s_mov_b32 m0, %0\n\ts_nop 0\n\tbuffer_load_dwordx4 %1, %2, %3 offen lds"                   \
               :: "s"((unsigned)(ldsa)), "v"((unsigned)(vo)), "s"(rs), "s"((unsigned)(so)) : "memory")
#define WAIT_VM(n) asm volatile("s_waitcnt vmcnt(%0)" :: "n"(n) : "memory")

__device__ __forceinline__ unsigned pack_bf16(float lo, float hi) {
  f32x2 v; v.x = lo; v.y = hi;
  bf16x2_t r = __builtin_convertvector(v, bf16x2_t);
  return __builtin_bit_cast(unsigned, r);
}
__device__ __forceinline__ u32x4 make_srd(const void* base, unsigned bytes) {
  const uintptr_t a = (uintptr_t)base;
  return u32x4{(unsigned)a, (unsigned)(a >> 32) & 0xffffu, bytes, 0x00020000u};
}
__device__ __forceinline__ int crow(int reg, int h) { return (reg & 3) + 8 * (reg >> 2) + 4 * h; }
__device__ __forceinline__ int launder(int v) { asm volatile("" : "+v"(v)); return v; }
__device__ __forceinline__ void lds_barrier() { asm volatile("s_waitcnt lgkmcnt(0)\n\ts_barrier" ::: "memory"); }

__device__ __forceinline__ int wave_incl_scan(int x) {
  x += __builtin_amdgcn_update_dpp(0, x, 0x111, 0xf, 0xf, false);
  x += __builtin_amdgcn_update_dpp(0, x, 0x112, 0xf, 0xf, false);
  x += __builtin_amdgcn_update_dpp(0, x, 0x114, 0xf, 0xf, false);
  x += __builtin_amdgcn_update_dpp(0, x, 0x118, 0xf, 0xf, false);
  x += __builtin_amdgcn_update_dpp(0, x, 0x142, 0xa, 0xf, false);
  x += __builtin_amdgcn_update_dpp(0, x, 0x143, 0xc, 0xf, false);
  return x;
}
__device__ __forceinline__ unsigned earow_bits(unsigned r) { return ((r & 7u) << 6) | ((r >> 3) << 23); }

constexpr int SORT_BLOCKS = NP;
constexpr int GEMM_BLOCKS = 208;
constexpr int WPREP_BLOCKS = 1;

struct SortLds {
  u32x4 raw[CH * 2];
  unsigned cs[NG * NKEY / 2 + 4];
  unsigned img[NG * 16];
  unsigned short inv[NG * FIX];
  unsigned short stage[CH];
  unsigned wsum[4];
};
struct GemmLds {
  unsigned short w[2][2][128][32];
  unsigned char xt[128 * 272];
};
union PrepLds { SortLds s; GemmLds g; };
static_assert(sizeof(PrepLds) <= 81920, "two workgroups per CU");

__global__ __launch_bounds__(512, 4) void k_prep(const unsigned short* __restrict__ x, const unsigned short* __restrict__ e_attr,
                                                 const int* __restrict__ src, const int* __restrict__ dst,
                                                 const unsigned short* __restrict__ u_w, const unsigned short* __restrict__ v_w,
                                                 const unsigned short* __restrict__ g_w, char* __restrict__ ws) {
  __shared__ __attribute__((aligned(16))) PrepLds lds;
  const int tid = threadIdx.x, lane = tid & 63;
  const int wv = __builtin_amdgcn_readfirstlane(tid >> 6);
  const int blk = blockIdx.x;
  STAMP(0, 0);
  if (blk < SORT_BLOCKS) {
    const int pidx = blk;
    const int nedge = N_EDGES - pidx * CH < CH ? N_EDGES - pidx * CH : CH;
    const bool have = tid * 4 < nedge;
    int4 dv = make_int4(0, 0, 0, 0), sv = make_int4(0, 0, 0, 0);
    if (have) { dv = *(const int4*)(dst + pidx * CH + tid * 4); sv = *(const int4*)(src + pidx * CH + tid * 4); }
    if (tid < 384) ((u32x4*)lds.s.cs)[tid] = u32x4{0, 0, 0, 0};
    if (tid < 256) { ((u32x4*)lds.s.inv)[tid] = u32x4{0, 0, 0, 0}; lds.s.img[tid * 16 + 3] = 0u; }
    {
      const u32x4 rsE = make_srd(e_attr, N_EDGES * 32);
      DMA16(rsE, &lds.s.raw[wv * 64], lane * 16, pidx * (CH * 32) + wv * 1024);
      DMA16(rsE, &lds.s.raw[(wv + 8) * 64], lane * 16, pidx * (CH * 32) + (wv + 8) * 1024);
    }
    lds_barrier();
    STAMP(0, 1);
    unsigned rec[4], key[4], rk[4];
    if (have) {
      const int d4[4] = {dv.x, dv.y, dv.z, dv.w}, s4[4] = {sv.x, sv.y, sv.z, sv.w};
#pragma unroll
      for (int k = 0; k < 4; ++k) {
        unsigned d = (unsigned)d4[k]; d = d < (unsigned)N_NODES ? d : (unsigned)(N_NODES - 1);
        unsigned s = (unsigned)s4[k]; s = s < (unsigned)N_NODES ? s : (unsigned)(N_NODES - 1);
        const unsigned g = d / GN, dn = d - g * GN;
        const unsigned b = dn == 0 ? 0u : (dn - 1) >> 3;
        const unsigned sh = s >= (unsigned)SRC_HALF ? 1u : 0u;
        key[k] = g * NKEY + b * 2 + sh;
        rec[k] = (s << 9) | dn;
        rk[k] = (atomicAdd(&lds.s.cs[g * 6 + b], 1u << (sh * 16)) >> (sh * 16)) & 0xffffu;
      }
    }
    {
      const u32x4 rsE = make_srd(e_attr, N_EDGES * 32);
#pragma unroll
      for (int i = 2; i < 6; ++i) DMA16(rsE, &lds.s.raw[(wv + 8 * i) * 64], lane * 16, pidx * (CH * 32) + (wv + 8 * i) * 1024);
      if (wv == 0) DMA16(rsE, &lds.s.raw[48 * 64], lane * 16, pidx * (CH * 32) + 48 * 1024);
    }
    STAMP(0, 2);
    lds_barrier();
    unsigned c6[6], gsum = 0, ginc = 0;
    if (tid < NG) {
#pragma unroll
      for (int i = 0; i < 6; ++i) { c6[i] = lds.s.cs[tid * 6 + i]; gsum += (c6[i] & 0xffffu) + (c6[i] >> 16); }
      ginc = (unsigned)wave_incl_scan((int)gsum);
      if (lane == 63) lds.s.wsum[wv] = ginc;
    }
    lds_barrier();
    if (tid < NG) {
      const u32x4 wsv = *(const u32x4*)lds.s.wsum;
      unsigned run = ginc - gsum + (wv > 0 ? wsv.x : 0u) + (wv > 1 ? wsv.y : 0u) + (wv > 2 ? wsv.z : 0u);
      const unsigned gstart = run;
      unsigned hd[2] = {0u, 0u};
#pragma unroll
      for (int i = 0; i < 6; ++i) {
        const unsigned s0 = run, s1 = run + (c6[i] & 0xffffu);
        run = s1 + (c6[i] >> 16);
        lds.s.cs[tid * 6 + i] = s0 | (s1 << 16);
        unsigned a = s1 - gstart, b = run - gstart;
        a = a < (unsigned)SLOT_REC ? a : (unsigned)SLOT_REC; b = b < (unsigned)SLOT_REC ? b : (unsigned)SLOT_REC;
        hd[i / 3] |= (a | (b << 5)) << (10 * (i % 3));
      }
      if (tid == NG - 1) lds.s.cs[NG * 6] = run;
      lds.s.img[tid * 16 + 0] = hd[0];
      lds.s.img[tid * 16 + 1] = hd[1];
      lds.s.img[tid * 16 + 2] = gstart | (gsum << 11);
    }
    STAMP(0, 3);
    lds_barrier();
    STAMP(0, 4);
    const unsigned short* start = (const unsigned short*)lds.s.cs;
    unsigned st[4], n[4], gs[4], nmax = 0;
    if (have) {
#pragma unroll
      for (int k = 0; k < 4; ++k) {
        st[k] = start[key[k]];
        n[k] = start[key[k] + 1] - st[k];
        gs[k] = start[(key[k] / NKEY) * NKEY];
        nmax = n[k] > nmax ? n[k] : nmax;
        lds.s.stage[st[k] + rk[k]] = (unsigned short)(tid * 4 + k);
      }
    }
    lds_barrier();
    unsigned xpos[4] = {~0u, ~0u, ~0u, ~0u};
    if (have) {
      unsigned rank[4] = {0u, 0u, 0u, 0u};
      if (nmax > 1) {
        for (unsigned i = 0; i < nmax; ++i) {
#pragma unroll
          for (int k = 0; k < 4; ++k) {
            const unsigned v = lds.s.stage[st[k] + (i < n[k] ? i : 0u)];
            rank[k] += (i < n[k] && v < (unsigned)(tid * 4 + k)) ? 1u : 0u;
          }
        }
      }
      unsigned* ovf = (unsigned*)(ws + WS_OVF) + (size_t)pidx * CH;
#pragma unroll
      for (int k = 0; k < 4; ++k) {
        const unsigned P = st[k] + rank[k];
        const unsigned idx = P - gs[k];
        const unsigned g = key[k] / NKEY, k12 = key[k] - g * NKEY;
        if (idx < (unsigned)FIX) {
          lds.s.img[g * 16 + 4 + idx] = rec[k] | (idx << 6) | ((unsigned)pidx << 23);
          atomicOr(&lds.s.img[g * 16 + 3], k12 << (4 * idx));
          lds.s.inv[g * FIX + idx] = (unsigned short)(tid * 4 + k);
        } else {
          ovf[P] = rec[k] | (k12 << 23);
          xpos[k] = P;
        }
      }
    }
    STAMP(0, 5);
    WAIT_VM(0);
    lds_barrier();
    STAMP(0, 6);
    {
      u32x4* so = (u32x4*)(ws + WS_SL) + (size_t)pidx * NG * 4;
      so[tid] = ((const u32x4*)lds.s.img)[tid];
      so[tid + 512] = ((const u32x4*)lds.s.img)[tid + 512];
      u32x4* ef = (u32x4*)(ws + WS_EAF) + (size_t)pidx * NG * 16;
      unsigned lr[8];
#pragma unroll
      for (int i = 0; i < 8; ++i) lr[i] = lds.s.inv[(tid >> 1) + 256 * i];
#pragma unroll
      for (int i = 0; i < 8; ++i) ef[tid + 512 * i] = lds.s.raw[lr[i] * 2 + (tid & 1)];
      u32x4* eo = (u32x4*)(ws + WS_EAS) + (size_t)pidx * CH * 2;
#pragma unroll
      for (int k = 0; k < 4; ++k) {
        if (xpos[k] != ~0u) {
          eo[xpos[k] * 2] = lds.s.raw[(tid * 4 + k) * 2];
          eo[xpos[k] * 2 + 1] = lds.s.raw[(tid * 4 + k) * 2 + 1];
        }
      }
    }
    STAMP(0, 8);
  } else if (blk < SORT_BLOCKS + GEMM_BLOCKS) {
    const int gb = blk - SORT_BLOCKS;
    const int j = gb >> 3;
    const int rg = (gb & 7) + 8 * (j >> 1), cp = j & 1;
    if (rg * 128 >= N_NODES) return;
    const int m = lane & 31, h = lane >> 5;
    const int cbl = wv & 1, cb = cp * 2 + cbl;
    const int rbl = wv >> 1;
    const int row0 = rg * 128 + rbl * 32;
    u32x4 wld[8], xld[4];
#pragma unroll
    for (int i = 0; i < 4; ++i) {
      const int q = tid + 512 * i;
      int xr = rg * 128 + (q >> 4); xr = xr < N_NODES ? xr : N_NODES - 1;
      xld[i] = *(const u32x4*)(x + (size_t)xr * DM + (q & 15) * 8);
    }
#pragma unroll
    for (int i = 0; i < 8; ++i) {
      const int q = tid + 512 * (i & 1);
      const unsigned short* W = (i >> 1) == 0 ? g_w + 128 * DM : ((i >> 1) == 1 ? v_w : ((i >> 1) == 2 ? g_w : u_w));
      wld[i] = *(const u32x4*)(W + (size_t)(q >> 3) * DM + cp * 64 + (q & 7) * 8);
    }
    __builtin_amdgcn_sched_barrier(0);
#pragma unroll
    for (int i = 0; i < 4; ++i) {
      const int q = tid + 512 * i;
      *(u32x4*)(lds.g.xt + (q >> 4) * 272 + (q & 15) * 16) = xld[i];
    }
#pragma unroll
    for (int i = 0; i < 4; ++i) {
      const int q = tid + 512 * (i & 1);
      *(u32x4*)&lds.g.w[i >> 1][(q >> 2) & 1][q >> 3][(q & 3) * 8] = wld[i];
    }
    STAMP(0, 1);
    lds_barrier();
    STAMP(0, 2);
    bf16x8 xa[8];
#pragma unroll
    for (int s = 0; s < 8; ++s) xa[s] = *(const bf16x8*)(lds.g.xt + (rbl * 32 + m) * 272 + s * 32 + h * 16);
    const int q4 = (lane & 15) >> 2, p4 = lane & 3, blk16 = (lane >> 4) & 1;
    const unsigned goff = (unsigned)(row0 + 4 * h) * 512u + (unsigned)(cb * 32 + m) * 4u;
#pragma unroll
    for (int pass = 0; pass < 2; ++pass) {
      f32x16 acc0, acc1;
#pragma unroll
      for (int i = 0; i < 16; ++i) { acc0[i] = 0.f; acc1[i] = 0.f; }
#pragma unroll
      for (int s = 0; s < 8; ++s) {
        const int kr = 16 * s + 8 * h + q4;
        s16x4 b0 = __builtin_amdgcn_ds_read_tr16_b64_v4i16((lds_s16x4_ptr)&lds.g.w[0][cbl][kr][blk16 * 16 + p4 * 4]);
        s16x4 b1 = __builtin_amdgcn_ds_read_tr16_b64_v4i16((lds_s16x4_ptr)&lds.g.w[0][cbl][kr + 4][blk16 * 16 + p4 * 4]);
        s16x4 v0 = __builtin_amdgcn_ds_read_tr16_b64_v4i16((lds_s16x4_ptr)&lds.g.w[1][cbl][kr][blk16 * 16 + p4 * 4]);
        s16x4 v1 = __builtin_amdgcn_ds_read_tr16_b64_v4i16((lds_s16x4_ptr)&lds.g.w[1][cbl][kr + 4][blk16 * 16 + p4 * 4]);
        acc0 = MFMA32(xa[s], __builtin_shufflevector(b0, b1, 0, 1, 2, 3, 4, 5, 6, 7), acc0);
        acc1 = MFMA32(xa[s], __builtin_shufflevector(v0, v1, 0, 1, 2, 3, 4, 5, 6, 7), acc1);
      }
      if (pass == 0) {
        lds_barrier();
#pragma unroll
        for (int i = 0; i < 4; ++i) {
          const int q = tid + 512 * (i & 1);
          *(u32x4*)&lds.g.w[i >> 1][(q >> 2) & 1][q >> 3][(q & 3) * 8] = wld[4 + i];
        }
        STAMP(0, 3);
        const __amdgpu_buffer_rsrc_t rsG = __builtin_amdgcn_make_buffer_rsrc((void*)(ws + WS_G), 0, (int)TAB_BYTES, 0x00020000);
#pragma unroll
        for (int r = 0; r < 16; r += 2) {
          const unsigned hvp = pack_bf16(acc1[r], acc1[r + 1]);
          float i0 = __builtin_amdgcn_rcpf(__uint_as_float(hvp << 16));
          float i1 = __builtin_amdgcn_rcpf(__uint_as_float(hvp & 0xffff0000u));
          i0 = __builtin_amdgcn_fmed3f(i0, -3.3895313892515355e38f, 3.3895313892515355e38f);
          i1 = __builtin_amdgcn_fmed3f(i1, -3.3895313892515355e38f, 3.3895313892515355e38f);
          const unsigned ip = pack_bf16(i0, i1);
#pragma unroll
          for (int k = 0; k < 2; ++k) {
            const unsigned L = k ? ip >> 16 : ip & 0xffffu;
            const float bsc = acc0[r + k] * NLOG2E;
            const unsigned mg = __float_as_uint(__builtin_amdgcn_fmed3f(__builtin_fabsf(bsc), 1.17549435e-38f, 3.37e38f));
            const unsigned t = mg + 0x8000u - L;
            const unsigned sl = (__float_as_uint(bsc) & 0x80000000u) | L;
            __builtin_amdgcn_raw_buffer_store_b32((t & 0x7fff0000u) | sl, rsG, goff + (unsigned)crow(r + k, 0) * 512u, 0, 0);
          }
        }
        lds_barrier();
      } else {
        const __amdgpu_buffer_rsrc_t rsA = __builtin_amdgcn_make_buffer_rsrc((void*)(ws + WS_AH), 0, (int)TAB_BYTES, 0x00020000);
#pragma unroll
        for (int r = 0; r < 16; ++r)
          __builtin_amdgcn_raw_buffer_store_b32(pack_bf16(acc0[r] * NLOG2E, acc1[r]), rsA, goff + (unsigned)crow(r, 0) * 512u, 0, 0);
      }
    }
    STAMP(0, 4);
  } else {
    u32x4* wf = (u32x4*)(ws + WS_WF);
    const int l = tid & 63;
    if (tid < 256) {
      const int cb = tid >> 6;
      unsigned hi[4], lo[4];
#pragma unroll
      for (int jj = 0; jj < 4; ++jj) {
        const float w0 = __uint_as_float((unsigned)g_w[(size_t)(256 + 8 * (l >> 5) + 2 * jj) * DM + cb * 32 + (l & 31)] << 16) * NLOG2E;
        const float w1 = __uint_as_float((unsigned)g_w[(size_t)(256 + 8 * (l >> 5) + 2 * jj + 1) * DM + cb * 32 + (l & 31)] << 16) * NLOG2E;
        hi[jj] = pack_bf16(w0, w1);
        lo[jj] = pack_bf16(w0 - __uint_as_float(hi[jj] << 16), w1 - __uint_as_float(hi[jj] & 0xffff0000u));
      }
      wf[(cb * 2 + 0) * 64 + l] = u32x4{hi[0], hi[1], hi[2], hi[3]};
      wf[(cb * 2 + 1) * 64 + l] = u32x4{lo[0], lo[1], lo[2], lo[3]};
    } else if (tid < 320) {
      const int dl = l >> 1, hh = l & 1;
      u32x4 v = u32x4{0, 0, 0, 0};
      if (dl < 16 && ((dl >> 2) & 1) == hh) {
        const int j = ((dl >> 3) << 2) | (dl & 3);
        const unsigned one = (j & 1) ? 0x3f800000u : 0x00003f80u;
        v.x = (j >> 1) == 0 ? one : 0u; v.y = (j >> 1) == 1 ? one : 0u; v.z = (j >> 1) == 2 ? one : 0u; v.w = (j >> 1) == 3 ? one : 0u;
      }
      wf[512 + l] = v;
    }
  }
}

constexpr int SROW = 80;
constexpr int SMAT = 17 * SROW + 48;
constexpr int LISTN = BPG * BCAP;
constexpr int AH_DMA = 29;
struct EdgeLds {
  u32x4 ea[(NP * FIX + POOL) * 2];
  u32x4 sl[NP * 4];
  u32x4 ah[AH_DMA * 64];
  u32x4 lut[64];
  unsigned list[LISTN + 64];
  unsigned char smat[NW * SMAT];
  short adj[NP * 16];
  unsigned exent[POOL];
  unsigned short expos[POOL];
  unsigned tot[8];
  unsigned seg[8];
  unsigned long long omask[4];
  unsigned poolcur, slow;
};
static_assert(sizeof(EdgeLds) <= 163840, "LDS");

__global__ __launch_bounds__(NT2) void k_edge(const char* __restrict__ ws, unsigned short* __restrict__ out) {
  __shared__ __attribute__((aligned(16))) EdgeLds lds;
  const int tid = threadIdx.x, lane = tid & 63;
  const int wv = __builtin_amdgcn_readfirstlane(tid >> 6);
  const int grp = blockIdx.x;
  const int bkt = wv % BPG, hf = wv / BPG;
  const int c = lane & 31, h = lane >> 5;
  STAMP(1, 0);
  const u32x4 rsS = make_srd(ws + WS_SL, NP * NG * 64);
  const u32x4 rsF = make_srd(ws + WS_EAF, NP * NG * 256);
  const u32x4 rsA = make_srd(ws + WS_AH, (unsigned)TAB_BYTES);
  const u32x4 rsL = make_srd(ws + WS_WF, 576 * 16);
  const __amdgpu_buffer_rsrc_t rsW = __builtin_amdgcn_make_buffer_rsrc((void*)(ws + WS_WF), 0, 576 * 16, 0x00020000);
  const unsigned* ovf = (const unsigned*)(ws + WS_OVF);
  const u32x4* eas = (const u32x4*)(ws + WS_EAS);

  {
    const unsigned vl = (unsigned)lane * 16u;
    const unsigned vs = (unsigned)(lane >> 2) * (NG * 64) + (unsigned)(lane & 3) * 16u;
    const unsigned vf = (unsigned)(lane >> 4) * (NG * 256) + (unsigned)(lane & 15) * 16u;
    const unsigned ahb = (unsigned)grp * (GN * 512);
    const unsigned l_sl = (unsigned)(uintptr_t)(lds_ptr)lds.sl, l_ah = (unsigned)(uintptr_t)(lds_ptr)lds.ah;
    const unsigned l_ea = (unsigned)(uintptr_t)(lds_ptr)lds.ea, l_lut = (unsigned)(uintptr_t)(lds_ptr)lds.lut;
    const unsigned w = (unsigned)wv;
    {
      const bool c8 = wv < 8;
      const u32x4 rs = c8 ? rsS : rsA;
      const unsigned vo = c8 ? vs : vl;
      const unsigned l0 = c8 ? l_sl + w * 2048u : l_ah + (w - 8u) * 2048u;
      const unsigned s0 = c8 ? (w * 32u * NG + (unsigned)grp) * 64u : ahb + (w - 8u) * 2048u;
      const unsigned ds = c8 ? 16u * NG * 64u : 1024u;
      DMA16R(rs, l0, vo, s0);
      DMA16R(rs, l0 + 1024u, vo, s0 + ds);
    }
#pragma unroll
    for (int i = 0; i < 5; ++i) DMA16R(rsF, l_ea + (w + 12u * i) * 1024u, vf, ((w + 12u * i) * 4u * NG + (unsigned)grp) * 256u);
    {
      const bool c4 = wv < 4;
      const u32x4 rs = c4 ? rsF : rsA;
      DMA16R(rs, c4 ? l_ea + (w + 60u) * 1024u : l_ah + (w + 4u) * 1024u, c4 ? vf : vl,
             c4 ? ((w + 60u) * 4u * NG + (unsigned)grp) * 256u : ahb + (w + 4u) * 1024u);
    }
    DMA16R(rsA, l_ah + (w + 16u) * 1024u, vl, ahb + (w + 16u) * 1024u);
    {
      const bool c1 = wv == 1;
      const u32x4 rs = c1 ? rsL : rsA;
      DMA16R(rs, c1 ? l_lut : l_ah + 28u * 1024u, vl, c1 ? 512u * 16u : ahb + 28u * 1024u);
    }
  }
  unsigned char* smat = lds.smat + wv * SMAT;
  ((u32x4*)smat)[lane] = u32x4{0, 0, 0, 0};
  if (lane < 24) ((u32x4*)smat)[64 + lane] = u32x4{0, 0, 0, 0};
  if (tid < 2) lds.ea[NP * FIX * 2 + tid] = u32x4{0, 0, 0, 0};
  if (tid == 2) lds.poolcur = 1u;
  STAMP(1, 3);
  WAIT_VM(8 + NSTAMP(1));
  STAMP(1, 1);
  lds_barrier();
  STAMP(1, 2);

  constexpr int XU = 4;
  u32x4 xr[XU][2], xrec;
  int xn = 0, xbase = 0;
  unsigned xsrc = 0;
  if (wv < BPG) {
    u32x4 hd[4];
    unsigned ex = 0; bool big = false;
#pragma unroll
    for (int k = 0; k < 4; ++k) {
      hd[k] = lds.sl[(lane * 4 + k) * 4];
      const unsigned total = (hd[k].z >> 11) & 2047u;
      ex += total > (unsigned)FIX ? total - FIX : 0u;
      big |= total > (unsigned)SLOT_REC;
    }
    const bool slow = __any(big) || __builtin_amdgcn_readlane(wave_incl_scan((int)ex), 63) > POOL - 1;
    const unsigned clip = slow ? (unsigned)FIX : (unsigned)SLOT_REC;
    unsigned st[4], cn[4], sum = 0;
#pragma unroll
    for (int k = 0; k < 4; ++k) {
      const unsigned long long H = ((unsigned long long)hd[k].y << 30) | hd[k].x;
      const unsigned f = (unsigned)(H >> (10 * wv));
      unsigned s0 = wv ? (unsigned)(H >> (10 * wv - 5)) & 31u : 0u, s1 = f & 31, s2 = (f >> 5) & 31;
      s0 = s0 < clip ? s0 : clip; s1 = s1 < clip ? s1 : clip; s2 = s2 < clip ? s2 : clip;
      st[k] = s0 | (s1 << 16);
      cn[k] = (s1 - s0) | ((s2 - s1) << 16);
      sum += cn[k];
      if (wv == 0) {
        const unsigned long long mk = __ballot(slow && ((hd[k].z >> 11) & 2047u) > (unsigned)FIX);
        if (lane == 0) lds.omask[k] = mk;
      }
    }
    const unsigned inc = (unsigned)wave_incl_scan((int)sum);
    const unsigned totp = (unsigned)__builtin_amdgcn_readlane((int)inc, 63);
    unsigned run = inc - sum;
    const unsigned b0 = (unsigned)(wv * BCAP), b1 = b0 + (totp & 0xffffu);
#pragma unroll
    for (int k = 0; k < 4; ++k) {
      const unsigned a0 = ((run & 0xffffu) + b0 - (st[k] & 0xffffu)) & 0xffffu;
      const unsigned a1 = ((run >> 16) + b1 - (st[k] >> 16)) & 0xffffu;
      ((unsigned*)lds.adj)[(lane * 4 + k) * 8 + wv] = a0 | (a1 << 16);
      run += cn[k];
    }
    if (lane == 0) { lds.tot[wv] = (totp & 0xffffu) + (totp >> 16); lds.slow = slow ? 1u : 0u; }
  } else if (wv >= 8) {
    const int p = tid - 512;
    const unsigned h2 = lds.sl[p * 4].z;
    const unsigned total = (h2 >> 11) & 2047u;
    if (total > (unsigned)FIX && total <= (unsigned)SLOT_REC) {
      xn = (int)total - FIX;
      xbase = (int)atomicAdd(&lds.poolcur, (unsigned)xn);
      if (xbase + xn > POOL) xn = 0;
    }
    xsrc = (unsigned)p * CH + (h2 & 2047u) + FIX;
    const __amdgpu_buffer_rsrc_t rsE = __builtin_amdgcn_make_buffer_rsrc((void*)eas, 0, NP * CH * 32, 0x00020000);
    const __amdgpu_buffer_rsrc_t rsO = __builtin_amdgcn_make_buffer_rsrc((void*)ovf, 0, NP * CH * 4 + 64, 0x00020000);
    xrec = __builtin_amdgcn_raw_buffer_load_b128(rsO, xn > 0 ? xsrc * 4u : OOB, 0, 0);
#pragma unroll
    for (int k = 0; k < XU; ++k) {
      xr[k][0] = __builtin_amdgcn_raw_buffer_load_b128(rsE, k < xn ? (xsrc + k) * 32u : OOB, 0, 0);
      xr[k][1] = __builtin_amdgcn_raw_buffer_load_b128(rsE, k < xn ? (xsrc + k) * 32u + 16u : OOB, 0, 0);
    }
  }
  STAMP(1, 4);
  lds_barrier();
  STAMP(1, 5);

  int maxtot = 0;
#pragma unroll
  for (int b = 0; b < BPG; ++b) { const int t = (int)lds.tot[b]; maxtot = t > maxtot ? t : maxtot; }
  maxtot = __builtin_amdgcn_readfirstlane(maxtot);
  const bool windowed = maxtot > BCAP;
  if (wv < 8) {
    if (__builtin_expect(!windowed, 1)) {
      const int p = tid >> 1, j4 = (tid & 1) * 4;
      const u32x4 hd = lds.sl[p * 4];
      const u32x4 rc = lds.sl[p * 4 + 1 + (tid & 1)];
      const unsigned keys = hd.w >> (4 * j4);
      const int total = (int)((hd.z >> 11) & 2047u) - j4;
      const short* ar = &lds.adj[p * 16];
      const int a0 = ar[keys & 15], a1 = ar[(keys >> 4) & 15], a2 = ar[(keys >> 8) & 15], a3 = ar[(keys >> 12) & 15];
      lds.list[total > 0 ? a0 + j4 : LISTN + c] = rc.x;
      lds.list[total > 1 ? a1 + j4 + 1 : LISTN + c] = rc.y;
      lds.list[total > 2 ? a2 + j4 + 2 : LISTN + c] = rc.z;
      lds.list[total > 3 ? a3 + j4 + 3 : LISTN + c] = rc.w;
    }
  } else {
    if (__builtin_amdgcn_readfirstlane((int)lds.slow)) xn = 0;
    const short* ar = &lds.adj[(tid - 512) * 16];
    const unsigned r4[4] = {xrec.x, xrec.y, xrec.z, xrec.w};
#pragma unroll
    for (int k = 0; k < XU; ++k) {
      if (k < xn) {
        const int pos = (int)(unsigned short)(ar[(r4[k] >> 23) & 15] + FIX + k);
        const unsigned ent = (r4[k] & 0x7fffffu) | earow_bits((unsigned)(NP * FIX + xbase + k));
        lds.exent[xbase + k] = ent; lds.expos[xbase + k] = (unsigned short)(pos | ((r4[k] >> 24) << 13));
        if (!windowed) lds.list[pos] = ent;
        lds.ea[(NP * FIX + xbase + k) * 2] = xr[k][0];
        lds.ea[(NP * FIX + xbase + k) * 2 + 1] = xr[k][1];
      }
    }
    if (__builtin_expect(__any(xn > XU), 0)) {
      for (int k = XU; k < xn; ++k) {
        const unsigned r = ovf[xsrc + k];
        const int pos = (int)(unsigned short)(ar[(r >> 23) & 15] + FIX + k);
        const unsigned ent = (r & 0x7fffffu) | earow_bits((unsigned)(NP * FIX + xbase + k));
        lds.exent[xbase + k] = ent; lds.expos[xbase + k] = (unsigned short)(pos | ((r >> 24) << 13));
        if (!windowed) lds.list[pos] = ent;
        lds.ea[(NP * FIX + xbase + k) * 2] = eas[(size_t)(xsrc + k) * 2];
        lds.ea[(NP * FIX + xbase + k) * 2 + 1] = eas[(size_t)(xsrc + k) * 2 + 1];
      }
    }
  }
  STAMP(1, 6);

  const __amdgpu_buffer_rsrc_t rsG = __builtin_amdgcn_make_buffer_rsrc((void*)(ws + WS_G), 0, (int)TAB_BYTES, 0x00020000);
  bf16x8 wch[2], wcl[2];
#pragma unroll
  for (int cb = 0; cb < 2; ++cb) {
    wch[cb] = __builtin_bit_cast(bf16x8, __builtin_amdgcn_raw_buffer_load_b128(rsW, lane * 16, ((hf * 2 + cb) * 2 + 0) * 1024, 0));
    wcl[cb] = __builtin_bit_cast(bf16x8, __builtin_amdgcn_raw_buffer_load_b128(rsW, lane * 16, ((hf * 2 + cb) * 2 + 1) * 1024, 0));
  }
  const int node0 = bkt ? 8 * bkt + 1 : 0;
  const unsigned laneoff = (unsigned)(hf * 256 + c * 4);
  const unsigned spos = (unsigned)((c >> 4) * 32 + ((c >> 2) & 1) * 16 + ((((c >> 3) & 1) << 2) | (c & 3)) * 2);
  unsigned char* sw = smat + spos - node0 * SROW;
  const unsigned char* lutp = (const unsigned char*)lds.lut + h * 16 - node0 * 32;
  const unsigned char* srow = smat + (c & 15) * SROW + h * 16;
  const unsigned char* zr = (const unsigned char*)&lds.lut[40] + h * 16;
  const unsigned char* slo = c < 16 ? srow : zr;
  const unsigned char* shi = c < 16 ? zr : srow;
  const unsigned char* eab = (const unsigned char*)lds.ea + h * 16;
  bf16x8 A0, A1;
  f32x16 acc;

  int src_p = -1, win = 0;
  bool first = true;
  for (;;) {
    const int tl = launder(tid);
    if (__builtin_expect(windowed || src_p >= 0, 0)) {
      if (src_p < 0) {
        if (first) lds_barrier();
        const unsigned clip = lds.slow ? (unsigned)FIX : (unsigned)SLOT_REC;
        for (int i = tl; i < NP * FIX; i += NT2) {
          const int p = i >> 3, idx = i & 7;
          const u32x4 hd = lds.sl[p * 4];
          if ((unsigned)idx < ((hd.z >> 11) & 2047u)) {
            const int k12 = (hd.w >> (4 * idx)) & 15, b = k12 >> 1;
            const int pos = (int)(unsigned short)(lds.adj[p * 16 + k12] + idx) - b * BCAP - win * BCAP;
            if ((unsigned)pos < (unsigned)BCAP) lds.list[b * BCAP + pos] = ((const unsigned*)lds.sl)[p * 16 + 4 + idx];
          }
        }
        if (clip > (unsigned)FIX) {
          const int np = (int)lds.poolcur;
          for (int i = 1 + tl; i < np; i += NT2) {
            const int b = lds.expos[i] >> 13;
            const int pos = (int)(lds.expos[i] & 0x1fff) - b * BCAP - win * BCAP;
            if ((unsigned)pos < (unsigned)BCAP) lds.list[b * BCAP + pos] = lds.exent[i];
          }
        }
      } else {
        const unsigned h2 = lds.sl[src_p * 4].z;
        const unsigned sb = (unsigned)src_p * CH + (h2 & 2047u) + FIX + (unsigned)win * (POOL - 1);
        int sn = (int)((h2 >> 11) & 2047u) - FIX - win * (POOL - 1); sn = sn < POOL - 1 ? sn : POOL - 1;
        if (tl < 8) lds.tot[tl] = 0;
        __syncthreads();
        unsigned r = 0;
        if (tl < sn) { r = ovf[sb + tl]; atomicAdd(&lds.tot[(r >> 24) & 7], 1u); }
        __syncthreads();
        if (tl < sn) {
          const int b = (r >> 24) & 7;
          int sg = 0;
          for (int bb = 0; bb < b; ++bb) sg += (int)lds.tot[bb];
          lds.list[b * BCAP + tl - sg] = (r & 0x7fffffu) | earow_bits((unsigned)(NP * FIX + 1 + tl));
          lds.ea[(NP * FIX + 1 + tl) * 2] = eas[(size_t)(sb + tl) * 2];
          lds.ea[(NP * FIX + 1 + tl) * 2 + 1] = eas[(size_t)(sb + tl) * 2 + 1];
        }
      }
    }
    int mycnt = __builtin_amdgcn_readfirstlane((int)lds.tot[bkt]) - (src_p < 0 ? win * BCAP : 0);
    mycnt = mycnt < 0 ? 0 : (mycnt > BCAP ? BCAP : mycnt);
    if (hf == 0) {
      const int e = mycnt + (tl & 31);
      if (e < ((mycnt + 31) & ~31)) lds.list[bkt * BCAP + e] = (unsigned)(node0 + 16) | earow_bits(NP * FIX);
    }
    const int ntile = (mycnt + 31) >> 5;
    WAIT_VM(4);
    lds_barrier();
    STAMP(1, 7);
    if (first) {
      const unsigned* ab = (const unsigned*)lds.ah + (node0 + 4 * h) * 128 + hf * 64 + c;
      unsigned v[16];
#pragma unroll
      for (int r = 0; r < 16; ++r) v[r] = ab[crow(r & 7, 0) * 128 + (r >> 3) * 32];
      u32x4 a0, a1;
#pragma unroll
      for (int k = 0; k < 4; ++k) {
        a0[k] = __builtin_amdgcn_perm(v[2 * k + 1], v[2 * k], 0x05040100u);
        a1[k] = __builtin_amdgcn_perm(v[8 + 2 * k + 1], v[8 + 2 * k], 0x05040100u);
      }
      A0 = __builtin_bit_cast(bf16x8, a0); A1 = __builtin_bit_cast(bf16x8, a1);
#pragma unroll
      for (int r = 0; r < 16; ++r) acc[r] = __uint_as_float(v[r] & 0xffff0000u);
      first = false;
    }

    if (ntile > 0) {
      __builtin_amdgcn_s_setprio(3);
      const u32x4* l4 = (const u32x4*)&lds.list[bkt * BCAP] + h;
      const unsigned* l1 = &lds.list[bkt * BCAP + c];
      const int last = ntile - 1;
#define EA_OFF(e) ((((e) >> 15) & 0x1ff00u) | (((e) >> 1) & 0xe0u))
      unsigned ad[16];
#pragma unroll
      for (int qd = 0; qd < 4; ++qd) {
        const u32x4 ro = l4[2 * qd];
        ad[4 * qd + 0] = (ro.x & 0x7ffe00u) | laneoff; ad[4 * qd + 1] = (ro.y & 0x7ffe00u) | laneoff;
        ad[4 * qd + 2] = (ro.z & 0x7ffe00u) | laneoff; ad[4 * qd + 3] = (ro.w & 0x7ffe00u) | laneoff;
      }
      const unsigned e0 = l1[0];
      unsigned dn = e0 & 63u;
      f32x16 G0, G1;
      __builtin_amdgcn_sched_barrier(0);
#pragma unroll
      for (int r = 0; r < 16; ++r) G0[r] = __uint_as_float(__builtin_amdgcn_raw_buffer_load_b32(rsG, ad[r], 0, 0));
      __builtin_amdgcn_sched_barrier(0);
#pragma unroll
      for (int r = 0; r < 16; ++r) G1[r] = __uint_as_float(__builtin_amdgcn_raw_buffer_load_b32(rsG, ad[r] + 128, 0, 0));
      bf16x8 oh = *(const bf16x8*)(lutp + dn * 32);
      bf16x8 ea = *(const bf16x8*)(eab + EA_OFF(e0));
      __builtin_amdgcn_sched_barrier(0);
      STAMP(1, 8);
      for (int it = 0; it < ntile; ++it) {
        const int nx = it < last ? it + 1 : last;
        {
          const int rem = ntile - it;
          if (rem == 3) __builtin_amdgcn_s_setprio(2);
          else if (rem == 2) __builtin_amdgcn_s_setprio(1);
          else if (rem == 1) __builtin_amdgcn_s_setprio(0);
        }
        u32x4 ro[4];
#pragma unroll
        for (int qd = 0; qd < 4; ++qd) ro[qd] = l4[8 * nx + 2 * qd];
        const unsigned en = l1[32 * nx];
        unsigned short* sp = (unsigned short*)(sw + dn * SROW);
        *sp = (unsigned short)0x3f80;
        __builtin_amdgcn_sched_barrier(0);
        {
          float ih[16];
#pragma unroll
          for (int r = 0; r < 16; ++r) ih[r] = __uint_as_float(__float_as_uint(G0[r]) << 16);
          G0 = MFMA32(ea, wch[0], G0);
          G0 = MFMA32(ea, wcl[0], G0);
          G0 = MFMA32(oh, A0, G0);
          asm volatile("" : "+v"(G0));
          __builtin_amdgcn_sched_barrier(0);
          const bf16x8 S0 = *(const bf16x8*)(slo);
          const bf16x8 S1 = *(const bf16x8*)(slo + 32);
#pragma unroll
          for (int qd = 0; qd < 4; ++qd) {
            ad[4 * qd + 0] = (ro[qd].x & 0x7ffe00u) | laneoff; ad[4 * qd + 1] = (ro[qd].y & 0x7ffe00u) | laneoff;
            ad[4 * qd + 2] = (ro[qd].z & 0x7ffe00u) | laneoff; ad[4 * qd + 3] = (ro[qd].w & 0x7ffe00u) | laneoff;
          }
          dn = en & 63u;
          __builtin_amdgcn_sched_barrier(0);
          unsigned P[8];
#pragma unroll
          for (int r = 0; r < 16; r += 2) {
            const float x0 = EXP2(G0[r]), x1 = EXP2(G0[r + 1]);
            const float m0 = RCP(__builtin_fmaf(x0, ih[r], ih[r]));
            const float m1 = RCP(__builtin_fmaf(x1, ih[r + 1], ih[r + 1]));
            P[r >> 1] = pack_bf16(m0, m1);
          }
#pragma unroll
          for (int r = 0; r < 16; ++r) G0[r] = GLOAD(ad[r]);
          acc = MFMA32(S0, __builtin_bit_cast(bf16x8, u32x4{P[0], P[1], P[2], P[3]}), acc);
          acc = MFMA32(S1, __builtin_bit_cast(bf16x8, u32x4{P[4], P[5], P[6], P[7]}), acc);
          asm volatile("" : "+v"(acc));
        }
        __builtin_amdgcn_sched_barrier(0);
        {
          float ih[16];
#pragma unroll
          for (int r = 0; r < 16; ++r) ih[r] = __uint_as_float(__float_as_uint(G1[r]) << 16);
          G1 = MFMA32(ea, wch[1], G1);
          G1 = MFMA32(ea, wcl[1], G1);
          G1 = MFMA32(oh, A1, G1);
          asm volatile("" : "+v"(G1));
          __builtin_amdgcn_sched_barrier(0);
          const bf16x8 S0 = *(const bf16x8*)(shi);
          const bf16x8 S1 = *(const bf16x8*)(shi + 32);
          *sp = (unsigned short)0;
          oh = *(const bf16x8*)(lutp + dn * 32);
          ea = *(const bf16x8*)(eab + EA_OFF(en));
          __builtin_amdgcn_sched_barrier(0);
          unsigned P[8];
#pragma unroll
          for (int r = 0; r < 16; r += 2) {
            const float x0 = EXP2(G1[r]), x1 = EXP2(G1[r + 1]);
            const float m0 = RCP(__builtin_fmaf(x0, ih[r], ih[r]));
            const float m1 = RCP(__builtin_fmaf(x1, ih[r + 1], ih[r + 1]));
            P[r >> 1] = pack_bf16(m0, m1);
          }
#pragma unroll
          for (int r = 0; r < 16; ++r) G1[r] = GLOAD(ad[r] + 128);
          acc = MFMA32(S0, __builtin_bit_cast(bf16x8, u32x4{P[0], P[1], P[2], P[3]}), acc);
          acc = MFMA32(S1, __builtin_bit_cast(bf16x8, u32x4{P[4], P[5], P[6], P[7]}), acc);
          asm volatile("" : "+v"(acc));
        }
        __builtin_amdgcn_sched_barrier(0);
      }
    }
    STAMP(1, 9);
    bool more = false;
    int nsrc = src_p, nwin = win + 1;
    if (src_p < 0) more = maxtot > nwin * BCAP;
    else more = (int)((lds.sl[src_p * 4].z >> 11) & 2047u) - FIX > nwin * (POOL - 1);
    if (!more) {
      nwin = 0;
      int best = NP;
#pragma unroll
      for (int k = 0; k < 4; ++k) {
        unsigned long long mk = lds.omask[k];
        const int lo = (src_p + 1 - k + 3) >> 2;
        mk = lo >= 64 ? 0ull : (mk >> lo) << lo;
        if (mk) { const int p = 4 * (int)__builtin_ctzll(mk) + k; best = p < best ? p : best; }
      }
      if (best < NP) { nsrc = best; more = true; }
    }
    nsrc = __builtin_amdgcn_readfirstlane(nsrc);
    more = __builtin_amdgcn_readfirstlane((int)more) != 0;
    if (__builtin_expect(!more, 1)) break;
    __syncthreads();
    src_p = nsrc; win = nwin;
  }
  STAMP(1, 12);
  {
    const int nb = bkt ? 8 : 9;
    const int l2 = launder(tid) & 63;
    const int cc = l2 & 31, hh = l2 >> 5;
    const __amdgpu_buffer_rsrc_t rsO = __builtin_amdgcn_make_buffer_rsrc((void*)out, 0, N_NODES * 256, 0x00020000);
    const unsigned ob = (unsigned)(grp * GN + node0 + 4 * hh) * 256u + (unsigned)(hf * 64 + cc) * 2u;
#pragma unroll
    for (int r = 0; r < 16; ++r) {
      const int dl0 = crow(r & 7, 0);
      if (dl0 <= 8) {
        const float z = acc[r];
        const float o = z * __builtin_amdgcn_rcpf(1.f + __builtin_amdgcn_exp2f(z * NLOG2E));
        const unsigned off = ob + (unsigned)dl0 * 256u + (unsigned)(r >> 3) * 64u;
        __builtin_amdgcn_raw_buffer_store_b16((unsigned short)pack_bf16(o, 0.f), rsO, (dl0 < 8 || (hh == 0 && nb == 9)) ? off : OOB, 0, 0);
      }
    }
  }
  STAMP(1, 13);
}

}

extern "C" void kernel_launch(void* const* d_in, const int* in_sizes, int n_in, void* d_out, int out_size,
                              void* d_ws, size_t ws_size, hipStream_t stream) {
  const unsigned short* x = (const unsigned short*)d_in[0];
  const unsigned short* e_attr = (const unsigned short*)d_in[1];
  const int* src = (const int*)d_in[2];
  const int* dst = (const int*)d_in[3];
  const unsigned short* u_w = (const unsigned short*)d_in[4];
  const unsigned short* v_w = (const unsigned short*)d_in[5];
  const unsigned short* g_w = (const unsigned short*)d_in[6];
  k_prep<<<SORT_BLOCKS + GEMM_BLOCKS + WPREP_BLOCKS, 512, 0, stream>>>(x, e_attr, src, dst, u_w, v_w, g_w, (char*)d_ws);
  k_edge<<<NG, NT2, 0, stream>>>((const char*)d_ws, (unsigned short*)d_out);
}
```

```cpp
#include <hip/hip_runtime.h>
#include <hip/hip_bf16.h>
#include <stdint.h>

namespace {

constexpr int N_NODES = 12500, N_EDGES = 400000, DM = 128;
constexpr int GN = 49;
constexpr int NG = 256;
constexpr int BPG = 6;
constexpr int NKEY = 12;
constexpr int SRC_HALF = 6250;
constexpr int NP = 256;
constexpr int CH = 1568;
constexpr int FIX = 8;
constexpr int SLOT_REC = 28;
constexpr int BCAP = 384;
constexpr int POOL = 192;
constexpr int BSLOTS = 6;
constexpr int NW = 12;
constexpr int NT2 = NW * 64;
constexpr float NLOG2E = -1.4426950408889634f;
constexpr unsigned OOB = 0x80000000u;

constexpr size_t TAB_BYTES = (size_t)N_NODES * 512;
constexpr size_t WS_G = 0;
constexpr size_t WS_SL = 6400000;
constexpr size_t WS_XR = WS_SL + (size_t)NP * NG * 64;
constexpr size_t WS_SP = WS_XR + (size_t)NP * NG * 256;
constexpr size_t WS_SPC = WS_SP + (size_t)NP * CH * 8;
constexpr size_t WS_WF = WS_SPC + 1024;

typedef short bf16x8 __attribute__((ext_vector_type(8)));
typedef short s16x4 __attribute__((ext_vector_type(4)));
typedef float f32x16 __attribute__((ext_vector_type(16)));
typedef float f32x2 __attribute__((ext_vector_type(2)));
typedef __bf16 bf16x2_t __attribute__((ext_vector_type(2)));
typedef unsigned u32x4 __attribute__((ext_vector_type(4)));
typedef unsigned u32x2 __attribute__((ext_vector_type(2)));
typedef __attribute__((address_space(3))) s16x4* lds_s16x4_ptr;
typedef __attribute__((address_space(3))) void* lds_ptr;

#define STAMP_DECL
#define STAMP_FLUSH(k) do {} while (0)
#define STAMP(k, i) do {} while (0)
#define NSTAMP(n) 0
#define GLOAD(a) __uint_as_float(__builtin_amdgcn_raw_buffer_load_b32(rsG, (a), 0, 0))
#define EXP2(x) __builtin_amdgcn_exp2f(x)
#define RCP(x) __builtin_amdgcn_rcpf(x)
#define MFMA32(a, b, c) __builtin_amdgcn_mfma_f32_32x32x16_bf16((a), (b), (c), 0, 0, 0)
#define DMA16(rs, ldsp, vo, so)                                                                               \
  asm volatile("s_mov_b32 m0, %0\n\ts_nop 0\n\tbuffer_load_dwordx4 %1, %2, %3 offen lds"                   \
               :: "s"((unsigned)(uintptr_t)(lds_ptr)(ldsp)), "v"((unsigned)(vo)), "s"(rs), "s"((unsigned)(so)) : "memory")
#define DMA16R(rs, ldsa, vo, so)                                                                              \
  asm volatile("s_mov_b32 m0, %0\n\ts_nop 0\n\tbuffer_load_dwordx4 %1, %2, %3 offen lds"                   \
               :: "s"((unsigned)(ldsa)), "v"((unsigned)(vo)), "s"(rs), "s"((unsigned)(so)) : "memory")
#define WAIT_VM(n) asm volatile("s_waitcnt vmcnt(%0)" :: "n"(n) : "memory")

__device__ __forceinline__ unsigned pack_bf16(float lo, float hi) {
  f32x2 v; v.x = lo; v.y = hi;
  bf16x2_t r = __builtin_convertvector(v, bf16x2_t);
  return __builtin_bit_cast(unsigned, r);
}
__device__ __forceinline__ u32x4 make_srd(const void* base, unsigned bytes) {
  const uintptr_t a = (uintptr_t)base;
  return u32x4{(unsigned)a, (unsigned)(a >> 32) & 0xffffu, bytes, 0x00020000u};
}
__device__ __forceinline__ int crow(int reg, int h) { return (reg & 3) + 8 * (reg >> 2) + 4 * h; }
__device__ __forceinline__ int launder(int v) { asm volatile("" : "+v"(v)); return v; }
__device__ __forceinline__ void lds_barrier() { asm volatile("s_waitcnt lgkmcnt(0)\n\ts_barrier" ::: "memory"); }

__device__ __forceinline__ void wait_flag_mask(unsigned* flag, unsigned mask, unsigned n) {
  const unsigned a = (unsigned)(uintptr_t)(lds_ptr)flag;
  for (;;) {
    unsigned v;
    asm volatile("ds_read_b32 %0, %1\n\ts_waitcnt lgkmcnt(0)" : "=v"(v) : "v"(a) : "memory");
    if (((unsigned)__builtin_amdgcn_readfirstlane((int)v) & mask) >= n) break;
    __builtin_amdgcn_s_sleep(1);
  }
}
__device__ __forceinline__ void wait_flag(unsigned* flag, unsigned n) { wait_flag_mask(flag, ~0u, n); }

__device__ __forceinline__ int wave_incl_scan(int x) {
  x += __builtin_amdgcn_update_dpp(0, x, 0x111, 0xf, 0xf, false);
  x += __builtin_amdgcn_update_dpp(0, x, 0x112, 0xf, 0xf, false);
  x += __builtin_amdgcn_update_dpp(0, x, 0x114, 0xf, 0xf, false);
  x += __builtin_amdgcn_update_dpp(0, x, 0x118, 0xf, 0xf, false);
  x += __builtin_amdgcn_update_dpp(0, x, 0x142, 0xa, 0xf, false);
  x += __builtin_amdgcn_update_dpp(0, x, 0x143, 0xc, 0xf, false);
  return x;
}
__device__ __forceinline__ unsigned local_bits(unsigned r) { return ((r & 7u) << 6) | ((r >> 3) << 23); }
__device__ __forceinline__ unsigned rec_eoff(unsigned rec, unsigned pbase) {
  return (pbase + (((rec >> 20) & 0x7f8u) | ((rec >> 6) & 7u))) * 32u;
}

constexpr int SORT_BLOCKS = NP;
constexpr int GEMM_BLOCKS = 208;
constexpr int WPREP_BLOCKS = 9;

struct SortLds {
  unsigned kc[NG * NKEY / 2];
  unsigned gc[NG];
  unsigned img[NG * 16];
  unsigned spc;
};
struct GemmLds {
  unsigned short w[2][2][128][32];
  unsigned char xt[128 * 272];
};
union PrepLds { SortLds s; GemmLds g; };
static_assert(sizeof(PrepLds) <= 81920, "two workgroups per CU");

__global__ __launch_bounds__(512, 4) void k_prep(const unsigned short* __restrict__ x, const unsigned short* __restrict__ e_attr,
                                                 const int* __restrict__ src, const int* __restrict__ dst,
                                                 const unsigned short* __restrict__ u_w, const unsigned short* __restrict__ v_w,
                                                 const unsigned short* __restrict__ g_w, char* __restrict__ ws) {
  __shared__ __attribute__((aligned(16))) PrepLds lds;
  const int tid = threadIdx.x, lane = tid & 63;
  const int wv = __builtin_amdgcn_readfirstlane(tid >> 6);
  const int blk = blockIdx.x;
  STAMP_DECL
  STAMP(0, 0);
  if (blk < SORT_BLOCKS) {
    const int pidx = blk;
    const int nedge = N_EDGES - pidx * CH < CH ? N_EDGES - pidx * CH : CH;
    const bool have = tid * 4 < nedge;
    int4 dv = make_int4(0, 0, 0, 0), sv = make_int4(0, 0, 0, 0);
    if (have) { dv = *(const int4*)(dst + pidx * CH + tid * 4); sv = *(const int4*)(src + pidx * CH + tid * 4); }
    if (tid < 448) ((u32x4*)lds.s.kc)[tid] = u32x4{0, 0, 0, 0};
    if (tid == 511) lds.s.spc = 0u;
    lds_barrier();
    STAMP(0, 1);
    if (have) {
      const int d4[4] = {dv.x, dv.y, dv.z, dv.w}, s4[4] = {sv.x, sv.y, sv.z, sv.w};
      unsigned g[4], dn[4], sc[4], idx[4];
#pragma unroll
      for (int k = 0; k < 4; ++k) {
        unsigned d = (unsigned)d4[k]; d = d < (unsigned)N_NODES ? d : (unsigned)(N_NODES - 1);
        sc[k] = (unsigned)s4[k]; sc[k] = sc[k] < (unsigned)N_NODES ? sc[k] : (unsigned)(N_NODES - 1);
        g[k] = d / GN; dn[k] = d - g[k] * GN;
        idx[k] = atomicAdd(&lds.s.gc[g[k]], 1u);
      }
      unsigned rk[4], k12[4];
#pragma unroll
      for (int k = 0; k < 4; ++k) {
        const unsigned b = dn[k] == 0 ? 0u : (dn[k] - 1) >> 3;
        const unsigned sh = sc[k] >= (unsigned)SRC_HALF ? 16u : 0u;
        k12[k] = b * 2 + (sh >> 4);
        rk[k] = (atomicAdd(&lds.s.kc[g[k] * 6 + b], idx[k] < (unsigned)SLOT_REC ? 1u << sh : 0u) >> sh) & 0xffffu;
      }
#pragma unroll
      for (int k = 0; k < 4; ++k) {
        const unsigned rec = (sc[k] << 9) | dn[k] | local_bits((unsigned)(tid * 4 + k));
        if (idx[k] < (unsigned)FIX) {
          lds.s.img[g[k] * 16 + 4 + idx[k]] = rec;
          ((unsigned short*)lds.s.img)[g[k] * 32 + 24 + idx[k]] = (unsigned short)(k12[k] | (rk[k] << 4));
        } else if (idx[k] < (unsigned)SLOT_REC) {
          ((u32x2*)(ws + WS_XR))[((size_t)pidx * NG + g[k]) * 32 + (idx[k] - FIX)] = u32x2{rec, k12[k] | (rk[k] << 4)};
        } else {
          const unsigned sp = atomicAdd(&lds.s.spc, 1u);
          ((u32x2*)(ws + WS_SP))[(size_t)pidx * CH + sp] = u32x2{rec, g[k]};
        }
      }
    }
    STAMP(0, 2);
    STAMP(0, 3);
    lds_barrier();
    STAMP(0, 4);
    if (tid < NG) {
      unsigned hd[2] = {0u, 0u};
#pragma unroll
      for (int i = 0; i < 6; ++i) {
        const unsigned cc = lds.s.kc[tid * 6 + i];
        hd[i / 3] |= ((cc & 31u) | ((cc >> 16) << 5)) << (10 * (i % 3));
      }
      ((u32x4*)lds.s.img)[tid * 4] = u32x4{hd[0], hd[1], lds.s.gc[tid], 0u};
      if (tid == 0) ((unsigned*)(ws + WS_SPC))[pidx] = lds.s.spc;
    }
    lds_barrier();
    {
      u32x4* so = (u32x4*)(ws + WS_SL) + (size_t)pidx * NG * 4;
      so[tid] = ((const u32x4*)lds.s.img)[tid];
      so[tid + 512] = ((const u32x4*)lds.s.img)[tid + 512];
    }
    STAMP(0, 8);
    STAMP_FLUSH(0);
  } else if (blk < SORT_BLOCKS + GEMM_BLOCKS) {
    const int gb = blk - SORT_BLOCKS;
    const int j = gb >> 3;
    const int rg = (gb & 7) + 8 * (j >> 1), cp = j & 1;
    if (rg * 128 >= N_NODES) return;
    const int m = lane & 31, h = lane >> 5;
    const int cbl = wv & 1, cb = cp * 2 + cbl;
    const int rbl = wv >> 1;
    const int row0 = rg * 128 + rbl * 32;
    u32x4 wld[4], xld[4];
#pragma unroll
    for (int i = 0; i < 4; ++i) {
      const int q = tid + 512 * i;
      int xr = rg * 128 + (q >> 4); xr = xr < N_NODES ? xr : N_NODES - 1;
      xld[i] = *(const u32x4*)(x + (size_t)xr * DM + (q & 15) * 8);
    }
#pragma unroll
    for (int i = 0; i < 4; ++i) {
      const int q = tid + 512 * (i & 1);
      const unsigned short* W = (i >> 1) == 0 ? g_w + 128 * DM : v_w;
      wld[i] = *(const u32x4*)(W + (size_t)(q >> 3) * DM + cp * 64 + (q & 7) * 8);
    }
    __builtin_amdgcn_sched_barrier(0);
#pragma unroll
    for (int i = 0; i < 4; ++i) {
      const int q = tid + 512 * i;
      *(u32x4*)(lds.g.xt + (q >> 4) * 272 + (q & 15) * 16) = xld[i];
    }
#pragma unroll
    for (int i = 0; i < 4; ++i) {
      const int q = tid + 512 * (i & 1);
      *(u32x4*)&lds.g.w[i >> 1][(q >> 2) & 1][q >> 3][(q & 3) * 8] = wld[i];
    }
    STAMP(0, 1);
    lds_barrier();
    STAMP(0, 2);
    bf16x8 xa[8];
#pragma unroll
    for (int s = 0; s < 8; ++s) xa[s] = *(const bf16x8*)(lds.g.xt + (rbl * 32 + m) * 272 + s * 32 + h * 16);
    const int q4 = (lane & 15) >> 2, p4 = lane & 3, blk16 = (lane >> 4) & 1;
    const unsigned goff = (unsigned)(row0 + 4 * h) * 512u + (unsigned)(cb * 32 + m) * 4u;
    {
      f32x16 acc0, acc1;
#pragma unroll
      for (int i = 0; i < 16; ++i) { acc0[i] = 0.f; acc1[i] = 0.f; }
#pragma unroll
      for (int s = 0; s < 8; ++s) {
        const int kr = 16 * s + 8 * h + q4;
        s16x4 b0 = __builtin_amdgcn_ds_read_tr16_b64_v4i16((lds_s16x4_ptr)&lds.g.w[0][cbl][kr][blk16 * 16 + p4 * 4]);
        s16x4 b1 = __builtin_amdgcn_ds_read_tr16_b64_v4i16((lds_s16x4_ptr)&lds.g.w[0][cbl][kr + 4][blk16 * 16 + p4 * 4]);
        s16x4 v0 = __builtin_amdgcn_ds_read_tr16_b64_v4i16((lds_s16x4_ptr)&lds.g.w[1][cbl][kr][blk16 * 16 + p4 * 4]);
        s16x4 v1 = __builtin_amdgcn_ds_read_tr16_b64_v4i16((lds_s16x4_ptr)&lds.g.w[1][cbl][kr + 4][blk16 * 16 + p4 * 4]);
        acc0 = MFMA32(xa[s], __builtin_shufflevector(b0, b1, 0, 1, 2, 3, 4, 5, 6, 7), acc0);
        acc1 = MFMA32(xa[s], __builtin_shufflevector(v0, v1, 0, 1, 2, 3, 4, 5, 6, 7), acc1);
      }
      STAMP(0, 3);
      const __amdgpu_buffer_rsrc_t rsG = __builtin_amdgcn_make_buffer_rsrc((void*)(ws + WS_G), 0, (int)TAB_BYTES, 0x00020000);
#pragma unroll
      for (int r = 0; r < 16; r += 2) {
        const unsigned hvp = pack_bf16(acc1[r], acc1[r + 1]);
        float i0 = __builtin_amdgcn_rcpf(__uint_as_float(hvp << 16));
        float i1 = __builtin_amdgcn_rcpf(__uint_as_float(hvp & 0xffff0000u));
        i0 = __builtin_amdgcn_fmed3f(i0, -3.3895313892515355e38f, 3.3895313892515355e38f);
        i1 = __builtin_amdgcn_fmed3f(i1, -3.3895313892515355e38f, 3.3895313892515355e38f);
        const unsigned ip = pack_bf16(i0, i1);
#pragma unroll
        for (int k = 0; k < 2; ++k) {
          const unsigned L = k ? ip >> 16 : ip & 0xffffu;
          const float bsc = acc0[r + k] * NLOG2E;
          const unsigned mg = __float_as_uint(__builtin_amdgcn_fmed3f(__builtin_fabsf(bsc), 1.17549435e-38f, 3.37e38f));
          const unsigned t = mg + 0x8000u - L;
          const unsigned sl = (__float_as_uint(bsc) & 0x80000000u) | L;
          __builtin_amdgcn_raw_buffer_store_b32((t & 0x7fff0000u) | sl, rsG, goff + (unsigned)crow(r + k, 0) * 512u, 0, 0);
        }
      }
    }
    STAMP(0, 4);
    STAMP_FLUSH(0);
  } else {
    u32x4* wf = (u32x4*)(ws + WS_WF);
    const int l = tid & 63;
    const int wb = blk - SORT_BLOCKS - GEMM_BLOCKS;
    if (wb > 0) {
      const int idx = (wb - 1) * 512 + tid;
      const int ks = (idx >> 6) & 7, cb = (idx >> 9) & 3;
      const unsigned short* W = (idx >> 11) ? u_w : g_w;
      unsigned v[8];
#pragma unroll
      for (int j = 0; j < 8; ++j) v[j] = W[(size_t)(16 * ks + 8 * (l >> 5) + j) * DM + cb * 32 + (l & 31)];
      wf[576 + idx] = u32x4{v[0] | (v[1] << 16), v[2] | (v[3] << 16), v[4] | (v[5] << 16), v[6] | (v[7] << 16)};
    } else if (tid < 256) {
      const int cb = tid >> 6;
      unsigned hi[4], lo[4];
#pragma unroll
      for (int jj = 0; jj < 4; ++jj) {
        const float w0 = __uint_as_float((unsigned)g_w[(size_t)(256 + 8 * (l >> 5) + 2 * jj) * DM + cb * 32 + (l & 31)] << 16) * NLOG2E;
        const float w1 = __uint_as_float((unsigned)g_w[(size_t)(256 + 8 * (l >> 5) + 2 * jj + 1) * DM + cb * 32 + (l & 31)] << 16) * NLOG2E;
        hi[jj] = pack_bf16(w0, w1);
        lo[jj] = pack_bf16(w0 - __uint_as_float(hi[jj] << 16), w1 - __uint_as_float(hi[jj] & 0xffff0000u));
      }
      wf[(cb * 2 + 0) * 64 + l] = u32x4{hi[0], hi[1], hi[2], hi[3]};
      wf[(cb * 2 + 1) * 64 + l] = u32x4{lo[0], lo[1], lo[2], lo[3]};
    } else if (tid < 320) {
      const int dl = l >> 1, hh = l & 1;
      u32x4 v = u32x4{0, 0, 0, 0};
      if (dl < 16 && ((dl >> 2) & 1) == hh) {
        const int j = ((dl >> 3) << 2) | (dl & 3);
        const unsigned one = (j & 1) ? 0x3f800000u : 0x00003f80u;
        v.x = (j >> 1) == 0 ? one : 0u; v.y = (j >> 1) == 1 ? one : 0u; v.z = (j >> 1) == 2 ? one : 0u; v.w = (j >> 1) == 3 ? one : 0u;
      }
      wf[512 + l] = v;
    }
  }
}

constexpr int SROW = 80;
constexpr int SMAT = 17 * SROW + 48;
constexpr int LISTN = BPG * BCAP;
#define GPF_ROWS 196
#define LDEPTH 4
constexpr int RROW = 272;
struct EdgeLds {
  u32x4 ea[(NP * FIX + POOL) * 2];
  u32x4 wop[8 * 64];
  u32x4 sl[NP * 4];
  unsigned char hu[58 * RROW];
  unsigned short adj[NP * NKEY];
  unsigned char ap[58 * RROW];
  u32x4 lut[64];
  unsigned list[LISTN + 32];
  unsigned char smat[NW * SMAT];
  unsigned tot[8];
  unsigned long long omask[4];
  unsigned short xdesc[256];
  unsigned tick[8];
  unsigned slow, sdone, bdone, ready;
  unsigned eaprog;
};
static_assert(sizeof(EdgeLds) <= 163840 - 1536, "LDS");
static_assert(NW * SMAT >= 13 * 1024, "x tile alias");

__global__ __launch_bounds__(NT2) void k_edge(const unsigned short* __restrict__ x, const unsigned short* __restrict__ e_attr,
                                              const char* __restrict__ ws, unsigned short* __restrict__ out) {
  __shared__ __attribute__((aligned(16))) EdgeLds lds;
  const int tid = threadIdx.x, lane = tid & 63;
  const int wv = __builtin_amdgcn_readfirstlane(tid >> 6);
  const int grp = (blockIdx.x & 7) * 32 + (blockIdx.x >> 3);
  const int bkt = (int)((0x545432231100ull >> (4 * wv)) & 15), par = (int)((0xc56u >> wv) & 1) ;
  const int partner = (int)((0x98ba45672301ull >> (4 * wv)) & 15);
  const int c = lane & 31, h = lane >> 5;
  STAMP_DECL
  STAMP(1, 0);
  const u32x4 rsS = make_srd(ws + WS_SL, NP * NG * 64);
  const u32x4 rsX = make_srd(x, N_NODES * 256);
  const u32x4 rsL = make_srd(ws + WS_WF, 576 * 16);
  const __amdgpu_buffer_rsrc_t rsW = __builtin_amdgcn_make_buffer_rsrc((void*)(ws + WS_WF), 0, (576 + 4096) * 16, 0x00020000);

  if (tid == 0) { lds.sdone = 0u; lds.bdone = 0u; lds.ready = 0u; lds.eaprog = 0u; }
  lds_barrier();
  const unsigned w = (unsigned)wv;
  unsigned char* smat = lds.smat + wv * SMAT;
  if (wv == 9) {
    const unsigned l_w = (unsigned)(uintptr_t)(lds_ptr)lds.wop, l_lut = (unsigned)(uintptr_t)(lds_ptr)lds.lut;
#pragma unroll
    for (int i = 0; i < 8; ++i) DMA16R(rsL, l_w + i * 1024u, (unsigned)lane * 16u, i * 1024u);
    DMA16R(rsL, l_lut, (unsigned)lane * 16u, 512u * 16u);
    WAIT_VM(0);
    if (lane == 0) atomicAdd(&lds.ready, 1u);
  }
  const u32x4 rsEs = make_srd(e_attr, N_EDGES * 32);
  if (wv >= 9) {
    const unsigned l_ea = (unsigned)(uintptr_t)(lds_ptr)lds.ea;
    const unsigned idx = ((unsigned)lane >> 1) & 7u;
    const unsigned* sp = (const unsigned*)lds.sl + ((w - 9u) * 4u + ((unsigned)lane >> 4)) * 16u;
    unsigned pb = ((w - 9u) * 4u + ((unsigned)lane >> 4)) * CH * 32u + ((unsigned)lane & 1u) * 16u;
    wait_flag_mask(&lds.sdone, 0x10000u, 0x10000u);
    STAMP(1, 2);
    unsigned char* prog = (unsigned char*)&lds.eaprog + (w - 9u);
#pragma unroll 1
    for (int i = 0; i < 22; i += 2) {
#pragma unroll
      for (int u = 0; u < 2; ++u) {
        unsigned n = (w - 9u) + 3u * (i + u);
        const bool in = n < 64u;
        const unsigned rec = sp[in ? (i + u) * 192 + 4 + idx : 0], total = in ? sp[(i + u) * 192 + 2] : 0u;
        const unsigned vo = idx < total ? pb + (((rec >> 20) & 0x7f8u) | ((rec >> 6) & 7u)) * 32u : OOB;
        if (in) DMA16R(rsEs, l_ea + n * 1024u, vo, 0u);
        pb += 12u * CH * 32u;
      }
      if (i >= LDEPTH && i < 20) { WAIT_VM(LDEPTH); if (lane == 0) *prog = (unsigned char)(i + 2 - LDEPTH); }
    }
    WAIT_VM(0); if (lane == 0) *prog = (unsigned char)22;
    STAMP(1, 4);
    wait_flag_mask(&lds.bdone, 255u, BPG + 1);
  }
  asm volatile("" ::: "memory");
  if (wv == 8) {
    if (lane < 2) lds.ea[NP * FIX * 2 + lane] = u32x4{0, 0, 0, 0};
    wait_flag_mask(&lds.sdone, 255u, 8);
    STAMP(1, 2);
    int xn[4], sum = 0;
    bool big = false;
#pragma unroll
    for (int k = 0; k < 4; ++k) {
      const unsigned total = ((const unsigned*)lds.sl)[(lane * 4 + k) * 16 + 2];
      const unsigned tc = total < (unsigned)SLOT_REC ? total : (unsigned)SLOT_REC;
      xn[k] = tc > (unsigned)FIX ? (int)tc - FIX : 0;
      sum += xn[k];
      big |= total > (unsigned)SLOT_REC;
    }
    const int inc = wave_incl_scan(sum);
    int npool = __builtin_amdgcn_readlane(inc, 63);
    const bool slow = npool > POOL - 1;
    if (slow) npool = 0;
    if (npool > 0) {
      const int p0 = inc - sum, p1 = p0 + xn[0], p2 = p1 + xn[1], p3 = p2 + xn[2];
#pragma unroll 1
      for (int i = 0; __any(i < xn[0] || i < xn[1] || i < xn[2] || i < xn[3]); ++i) {
        const int d = (lane << 7) | i;
        if (i < xn[0]) lds.xdesc[p0 + i] = (unsigned short)d;
        if (i < xn[1]) lds.xdesc[p1 + i] = (unsigned short)(d + 32);
        if (i < xn[2]) lds.xdesc[p2 + i] = (unsigned short)(d + 64);
        if (i < xn[3]) lds.xdesc[p3 + i] = (unsigned short)(d + 96);
      }
    }
    const __amdgpu_buffer_rsrc_t rsO = __builtin_amdgcn_make_buffer_rsrc((void*)(ws + WS_XR), 0, NP * NG * 256, 0x00020000);
    u32x2 xrec[3]; unsigned xd[3];
#pragma unroll
    for (int r = 0; r < 3; ++r) {
      xd[r] = lds.xdesc[lane + 64 * r];
      const unsigned e = ((xd[r] >> 5) * NG + (unsigned)grp) * 32u + (xd[r] & 31u);
      xrec[r] = __builtin_amdgcn_raw_buffer_load_b64(rsO, lane + 64 * r < npool ? e * 8u : OOB, 0, 0);
    }
    if (lane == 0) atomicAdd(&lds.sdone, 0x10000u);
    if (lane < 4) lds.omask[lane] = 0ull;
    if (__builtin_expect(__any(big), 0)) {
#pragma unroll
      for (int k = 0; k < 4; ++k) {
        const unsigned long long mk = __ballot(((const unsigned*)lds.sl)[(lane * 4 + k) * 16 + 2] > (unsigned)SLOT_REC);
        if (lane == 0) lds.omask[k] = mk;
      }
    }
    if (lane == 0) lds.slow = slow ? 1u : 0u;
    asm volatile("s_waitcnt lgkmcnt(0)" ::: "memory");
    if (lane == 0) atomicAdd(&lds.bdone, 1u);
    STAMP(1, 4);
    {
      const unsigned l_p = (unsigned)(uintptr_t)(lds_ptr)&lds.ea[(NP * FIX + 1) * 2];
#pragma unroll
      for (int r = 0; r < 3; ++r) {
        const unsigned eo = lane + 64 * r < npool ? rec_eoff(xrec[r].x, (xd[r] >> 5) * CH) : OOB;
#pragma unroll
        for (int q = 0; q < 2; ++q) {
          const unsigned v = (unsigned)__builtin_amdgcn_ds_bpermute((q * 32 + (lane >> 1)) * 4, (int)eo) + ((unsigned)lane & 1u) * 16u;
          if (64 * r + 32 * q < npool && (64 * r + 32 * q + 32 < POOL || lane < 62)) DMA16R(rsEs, l_p + (2 * r + q) * 1024u, v, 0u);
        }
      }
    }
    wait_flag_mask(&lds.bdone, 255u, BPG + 1);
    STAMP(1, 5);
#pragma unroll
    for (int r = 0; r < 3; ++r) {
      if (lane + 64 * r < npool) {
        const int pos = lds.adj[(xd[r] >> 5) * NKEY + (xrec[r].y & 15)] + (int)(xrec[r].y >> 4);
        if (pos < LISTN) lds.list[pos] = (xrec[r].x & 0x7ffe3fu) | local_bits((unsigned)(NP * FIX + 1 + lane + 64 * r));
      }
    }
    asm volatile("s_waitcnt lgkmcnt(0)" ::: "memory");
    if (lane == 0) atomicAdd(&lds.ready, 1u);
    WAIT_VM(0);
    if (lane == 0) ((unsigned char*)&lds.eaprog)[3] = (unsigned char)1;
  }
  asm volatile("" ::: "memory");
  if (wv < 8) {
    {
      const unsigned vl = (unsigned)lane * 16u;
      const unsigned vs = (unsigned)(lane >> 2) * (NG * 64) + (unsigned)(lane & 3) * 16u;
      const unsigned l_sl = (unsigned)(uintptr_t)(lds_ptr)lds.sl, l_xt = (unsigned)(uintptr_t)(lds_ptr)lds.smat;
      const unsigned xb = (unsigned)grp * (GN * 256);
      DMA16R(rsS, l_sl + w * 2048u, vs, (w * 32u * NG + (unsigned)grp) * 64u);
      DMA16R(rsS, l_sl + w * 2048u + 1024u, vs, ((w * 32u + 16u) * NG + (unsigned)grp) * 64u);
      const unsigned vx0 = (vl & 0x300u) | ((((unsigned)lane & 15u) ^ ((4u * w + ((unsigned)lane >> 4)) & 15u)) << 4);
      DMA16R(rsX, l_xt + w * 1024u, vx0, xb + w * 1024u);
      const unsigned n2 = wv < 5 ? w + 8u : w;
      DMA16R(rsX, l_xt + n2 * 1024u, vx0, xb + n2 * 1024u);
    }
    WAIT_VM(2);
    if (lane == 0) atomicAdd(&lds.sdone, 1u);
    bf16x8 wq[8];
#pragma unroll
    for (int ks = 0; ks < 8; ++ks)
      wq[ks] = __builtin_bit_cast(bf16x8, __builtin_amdgcn_raw_buffer_load_b128(rsW, (unsigned)lane * 16u, (576 + (wv * 8 + ks) * 64) * 16, 0));
    STAMP(1, 3);
    WAIT_VM(8);
    if (lane == 0) atomicAdd(&lds.sdone, 256u);
    STAMP(1, 1);
    wait_flag_mask(&lds.sdone, 255u, 8);
    STAMP(1, 2);
    if (wv < BPG) {
      unsigned cn[4], sum = 0;
#pragma unroll
      for (int k = 0; k < 4; ++k) {
        const unsigned f = ((const unsigned*)lds.sl)[(lane * 4 + k) * 16 + (wv >= 3)] >> (10 * (wv % 3));
        cn[k] = (f & 31u) | (((f >> 5) & 31u) << 16);
        sum += cn[k];
      }
      const unsigned inc = (unsigned)wave_incl_scan((int)sum);
      const unsigned totp = (unsigned)__builtin_amdgcn_readlane((int)inc, 63);
      unsigned run = inc - sum + ((unsigned)(wv * BCAP) | (((unsigned)(wv * BCAP) + (totp & 0xffffu)) << 16));
#pragma unroll
      for (int k = 0; k < 4; ++k) { ((unsigned*)lds.adj)[(lane * 4 + k) * 6 + wv] = run; run += cn[k]; }
      {
        const int tb = (int)((totp & 0xffffu) + (totp >> 16));
        if (lane == 0) { lds.tot[wv] = (unsigned)tb; lds.tick[wv] = 0u; }
        const int e = tb + c;
        if (e < ((tb + 31) & ~31) && e < BCAP) lds.list[wv * BCAP + e] = (unsigned)((wv ? 8 * wv + 1 : 0) + 16) | local_bits(NP * FIX);
      }
      asm volatile("s_waitcnt lgkmcnt(0)" ::: "memory");
      if (lane == 0) atomicAdd(&lds.bdone, 1u + 256u);
    }
    STAMP(1, 4);
    wait_flag(&lds.bdone, BPG * 256);
    STAMP(1, 5);
    {
      const int p = tid >> 1, j = tid & 1;
      const u32x4 rc = lds.sl[p * 4 + 1 + j];
      const u32x2 kr = ((const u32x2*)lds.sl)[p * 8 + 6 + j];
      const int total = (int)((const unsigned*)lds.sl)[p * 16 + 2] - 4 * j;
      const unsigned short* ar = &lds.adj[p * NKEY];
      int a0 = ar[kr.x & 15] + ((kr.x >> 4) & 31), a1 = ar[(kr.x >> 16) & 15] + ((kr.x >> 20) & 31);
      int a2 = ar[kr.y & 15] + ((kr.y >> 4) & 31), a3 = ar[(kr.y >> 16) & 15] + ((kr.y >> 20) & 31);
      a0 = (total > 0 && a0 < LISTN) ? a0 : LISTN + c; a1 = (total > 1 && a1 < LISTN) ? a1 : LISTN + c;
      a2 = (total > 2 && a2 < LISTN) ? a2 : LISTN + c; a3 = (total > 3 && a3 < LISTN) ? a3 : LISTN + c;
      const unsigned eb = ((unsigned)p << 23) | ((unsigned)j << 8);
      lds.list[a0] = (rc.x & 0x7ffe3fu) | eb;
      lds.list[a1] = (rc.y & 0x7ffe3fu) | (eb + 64u);
      lds.list[a2] = (rc.z & 0x7ffe3fu) | (eb + 128u);
      lds.list[a3] = (rc.w & 0x7ffe3fu) | (eb + 192u);
    }
    wait_flag_mask(&lds.sdone, 0xff00u, 8 * 256);
    const float sc = wv < 4 ? NLOG2E : 1.0f;
    unsigned char* dp = (wv < 4 ? lds.ap : lds.hu) + (4 * h) * RROW + ((wv & 3) * 32 + c) * 2;
#pragma unroll
    for (int rb = 0; rb < 2; ++rb) {
      int rr = rb * 32 + c; rr = rr < GN ? rr : GN - 1;
      const char* xa = (const char*)lds.smat + rr * 256;
      const int sw16 = (rr & 15) * 16;
      f32x16 ac;
#pragma unroll
      for (int i = 0; i < 16; ++i) ac[i] = 0.f;
#pragma unroll
      for (int ks = 0; ks < 8; ++ks) ac = MFMA32(*(const bf16x8*)(xa + ((ks * 32 + h * 16) ^ sw16)), wq[ks], ac);
#pragma unroll
      for (int r = 0; r < 16; r += 2) {
        const unsigned pk = pack_bf16(ac[r] * sc, ac[r + 1] * sc);
        if (rb * 32 + crow(r, 0) < 56 || (rb * 32 + crow(r, 0) == 56 && h == 0)) {
          *(unsigned short*)(dp + (rb * 32 + crow(r, 0)) * RROW) = (unsigned short)pk;
          *(unsigned short*)(dp + (rb * 32 + crow(r, 0) + 1) * RROW) = (unsigned short)(pk >> 16);
        }
      }
    }
    asm volatile("s_waitcnt lgkmcnt(0)" ::: "memory");
    if (lane == 0) atomicAdd(&lds.ready, 1u);
    wait_flag_mask(&lds.bdone, 255u, BPG + 1);
  }
  STAMP(1, 6);
  bool batch;
  {
    int maxtot = 0;
#pragma unroll
    for (int b = 0; b < BPG; ++b) { const int t = (int)lds.tot[b]; maxtot = t > maxtot ? t : maxtot; }
    batch = __builtin_amdgcn_readfirstlane((int)(maxtot > BCAP || lds.slow != 0u)) != 0;
  }

  const __amdgpu_buffer_rsrc_t rsG = __builtin_amdgcn_make_buffer_rsrc((void*)(ws + WS_G), 0, (int)TAB_BYTES, 0x00020000);
  const unsigned char* eab = (const unsigned char*)lds.ea + h * 16;
#define EA_OFF(e) ((((e) >> 15) & 0x1ff00u) | (((e) >> 1) & 0xe0u))
  const int node0 = bkt ? 8 * bkt + 1 : 0;
  const unsigned laneoff = (unsigned)(c * 4);
  const int sA = par * 256, sB = 256 - sA;
  const unsigned spos = (unsigned)((c >> 4) * 32 + ((c >> 2) & 1) * 16 + ((((c >> 3) & 1) << 2) | (c & 3)) * 2);
  unsigned char* sw = smat + spos - node0 * SROW;
  const unsigned char* lutp = (const unsigned char*)lds.lut + h * 16 - node0 * 32;
  const unsigned char* srow = smat + (c & 15) * SROW + h * 16;
  const unsigned char* zr = (const unsigned char*)&lds.lut[40] + h * 16;
  const unsigned char* slo = c < 16 ? srow : zr;
  const unsigned char* shi = c < 16 ? zr : srow;
  const unsigned char* wA = (const unsigned char*)lds.wop + lane * 16 + par * 4096;
  const unsigned char* wB = (const unsigned char*)lds.wop + lane * 16 + (1 - par) * 4096;
  const unsigned char* ab0 = lds.ap + (node0 + 4 * h + ((lane & 15) >> 2)) * RROW + (((lane >> 4) & 1) * 16 + (lane & 3) * 4) * 2;
  const unsigned char* aA = ab0 + par * 128;
  const unsigned char* aB = ab0 + (1 - par) * 128;
  f32x16 accA, accB;

  int mode = batch ? 1 : 0, rp = 0, rw = 0;
  bool first = true, allin = false;
  auto wait_rows = [&](unsigned e) __attribute__((always_inline)) {
    const unsigned a = (unsigned)(uintptr_t)(lds_ptr)&lds.eaprog;
    for (;;) {
      unsigned v;
      asm volatile("ds_read_b32 %0, %1\n\ts_waitcnt lgkmcnt(0)" : "=v"(v) : "v"(a) : "memory");
      const unsigned pv = (unsigned)__builtin_amdgcn_readfirstlane((int)v);
      if (pv == 0x01161616u) { allin = true; break; }
      unsigned m = pv & 255u, m1 = (pv >> 8) & 255u, m2 = (pv >> 16) & 255u;
      m = m < m1 ? m : m1; m = m < m2 ? m : m2;
      const unsigned hi = e >> 23;
      const bool pad = (e & 0xff8001c0u) == 0x80000000u;
      if (!__any(hi < 256u ? hi >= 12u * m : (!(pv >> 24) && !pad))) break;
      __builtin_amdgcn_s_sleep(2);
    }
  };
  for (;;) {
    const int tl = launder(tid);
    if (__builtin_expect(mode != 0, 0)) {
      WAIT_VM(0);
      __syncthreads();
      if (tl < 8) lds.tot[tl] = 0;
      __syncthreads();
      bool ok = false; unsigned rec = 0, pb = 0;
      if (mode == 1) {
        const int p = rp + tl / SLOT_REC, idx = tl % SLOT_REC;
        if (tl < BSLOTS * SLOT_REC && p < NP && (unsigned)idx < lds.sl[p * 4].z) {
          ok = true; pb = (unsigned)p * CH;
          rec = idx < FIX ? ((const unsigned*)lds.sl)[p * 16 + 4 + idx] : ((const u32x2*)(ws + WS_XR))[((size_t)p * NG + grp) * 32 + idx - FIX].x;
        }
      } else {
        const int i = rw * (POOL - 1) + tl;
        if (tl < POOL - 1 && i < (int)((const unsigned*)(ws + WS_SPC))[rp]) {
          const u32x2 sp = ((const u32x2*)(ws + WS_SP))[(size_t)rp * CH + i];
          if ((int)sp.y == grp) { ok = true; rec = sp.x; pb = (unsigned)rp * CH; }
        }
      }
      if (ok) {
        const unsigned dn = rec & 63u;
        const unsigned b = dn == 0 ? 0u : (dn - 1) >> 3;
        const unsigned pos = atomicAdd(&lds.tot[b], 1u);
        lds.list[b * BCAP + pos] = (rec & 0x7ffe3fu) | local_bits((unsigned)(NP * FIX + 1 + tl));
        const u32x4* row = (const u32x4*)((const char*)e_attr + rec_eoff(rec, pb));
        lds.ea[(NP * FIX + 1 + tl) * 2] = row[0];
        lds.ea[(NP * FIX + 1 + tl) * 2 + 1] = row[1];
      }
      __syncthreads();
      if (tl < BPG * 32) {
        const int b = tl >> 5, tb = (int)lds.tot[b], e = tb + (tl & 31);
        if (e < ((tb + 31) & ~31)) lds.list[b * BCAP + e] = (unsigned)((b ? 8 * b + 1 : 0) + 16) | local_bits(NP * FIX);
        if ((tl & 31) == 0) lds.tick[b] = 0u;
      }
      __syncthreads();
      allin = true;
    } else {
      wait_flag(&lds.ready, 10);
    }
    const int ntile = (__builtin_amdgcn_readfirstlane((int)lds.tot[bkt]) + 31) >> 5;
    STAMP(1, 7);
    if (first) {
      ((u32x4*)smat)[lane] = u32x4{0, 0, 0, 0};
      if (lane < 24) ((u32x4*)smat)[64 + lane] = u32x4{0, 0, 0, 0};
      const unsigned char* hb = lds.hu + (node0 + 4 * h) * RROW + (par * 64 + c) * 2;
#pragma unroll
      for (int r = 0; r < 16; ++r) {
        accA[r] = __uint_as_float((unsigned)*(const unsigned short*)(hb + crow(r & 7, 0) * RROW + (r >> 3) * 64) << 16);
        accB[r] = 0.f;
      }
      first = false;
    }

    const unsigned tka = (unsigned)(uintptr_t)(lds_ptr)&lds.tick[bkt];
#define GRAB(dst) asm volatile("ds_add_rtn_u32 %0, %1, %2" : "=v"(dst) : "v"(tka), "v"(lane == 0 ? 1u : 0u) : "memory")
    unsigned tkv;
    GRAB(tkv);
    asm volatile("s_waitcnt lgkmcnt(0)" : "+v"(tkv) :: "memory");
    const int t0 = __builtin_amdgcn_readfirstlane((int)tkv);
    if (t0 < ntile) {
      __builtin_amdgcn_s_setprio(3);
      const u32x4* l4 = (const u32x4*)&lds.list[bkt * BCAP] + h;
      const unsigned* l1 = &lds.list[bkt * BCAP + c];
      const int last = ntile - 1;
      unsigned ad[16];
#pragma unroll
      for (int qd = 0; qd < 4; ++qd) {
        const u32x4 ro = l4[8 * t0 + 2 * qd];
        ad[4 * qd + 0] = (ro.x & 0x7ffe00u) | laneoff; ad[4 * qd + 1] = (ro.y & 0x7ffe00u) | laneoff;
        ad[4 * qd + 2] = (ro.z & 0x7ffe00u) | laneoff; ad[4 * qd + 3] = (ro.w & 0x7ffe00u) | laneoff;
      }
      const unsigned e0 = l1[32 * t0];
      unsigned dn = e0 & 63u;
      f32x16 G0, G1;
      __builtin_amdgcn_sched_barrier(0);
#pragma unroll
      for (int r = 0; r < 16; ++r) G0[r] = __uint_as_float(__builtin_amdgcn_raw_buffer_load_b32(rsG, ad[r], sA, 0));
      __builtin_amdgcn_sched_barrier(0);
#pragma unroll
      for (int r = 0; r < 16; ++r) G1[r] = __uint_as_float(__builtin_amdgcn_raw_buffer_load_b32(rsG, ad[r] + 128, sA, 0));
      bf16x8 oh = *(const bf16x8*)(lutp + dn * 32);
      if (!allin) wait_rows(e0);
      bf16x8 ea = *(const bf16x8*)(eab + EA_OFF(e0));
      __builtin_amdgcn_sched_barrier(0);
      STAMP(1, 8);

#define PHASE(G, ACC, WP, AP, CB, SB, SOFF, IMM, MID)                                                              \
      {                                                                                                            \
        const bf16x8 whi = *(const bf16x8*)((WP) + (CB) * 2048);                                                   \
        const s16x4 a0 = __builtin_amdgcn_ds_read_tr16_b64_v4i16((lds_s16x4_ptr)((AP) + (CB) * 64));               \
        const s16x4 a1 = __builtin_amdgcn_ds_read_tr16_b64_v4i16((lds_s16x4_ptr)((AP) + (CB) * 64 + 8 * RROW));    \
        __builtin_amdgcn_sched_barrier(0);                                                                         \
        float ih[16];                                                                                              \
        _Pragma("unroll") for (int r = 0; r < 16; ++r) ih[r] = __uint_as_float(__float_as_uint(G[r]) << 16);      \
        G = MFMA32(ea, whi, G);                                                                                    \
        G = MFMA32(oh, __builtin_shufflevector(a0, a1, 0, 1, 2, 3, 4, 5, 6, 7), G);                                \
        asm volatile("" : "+v"(G));                                                                                \
        __builtin_amdgcn_sched_barrier(0);                                                                         \
        const bf16x8 S0 = *(const bf16x8*)(SB);                                                                    \
        const bf16x8 S1 = *(const bf16x8*)((SB) + 32);                                                             \
        MID                                                                                                        \
        __builtin_amdgcn_sched_barrier(0);                                                                         \
        unsigned P[8];                                                                                             \
        _Pragma("unroll") for (int r = 0; r < 16; r += 2) {                                                        \
          const float x0 = EXP2(G[r]), x1 = EXP2(G[r + 1]);                                                        \
          const float m0 = RCP(__builtin_fmaf(x0, ih[r], ih[r]));                                                  \
          const float m1 = RCP(__builtin_fmaf(x1, ih[r + 1], ih[r + 1]));                                          \
          P[r >> 1] = pack_bf16(m0, m1);                                                                           \
        }                                                                                                          \
        _Pragma("unroll") for (int r = 0; r < 16; ++r)                                                             \
          G[r] = __uint_as_float(__builtin_amdgcn_raw_buffer_load_b32(rsG, ad[r] + (IMM), (SOFF), 0));             \
        ACC = MFMA32(S0, __builtin_bit_cast(bf16x8, u32x4{P[0], P[1], P[2], P[3]}), ACC);                          \
        ACC = MFMA32(S1, __builtin_bit_cast(bf16x8, u32x4{P[4], P[5], P[6], P[7]}), ACC);                          \
        asm volatile("" : "+v"(ACC));                                                                              \
        __builtin_amdgcn_sched_barrier(0);                                                                         \
      }
      for (;;) {
        GRAB(tkv);
        unsigned short* sp = (unsigned short*)(sw + dn * SROW);
        *sp = (unsigned short)0x3f80;
        PHASE(G0, accA, wA, aA, 0, slo, sB, 0, )
        asm volatile("" : "+v"(tkv));
        const int nxt = __builtin_amdgcn_readfirstlane((int)tkv);
        const int nx = nxt < last ? nxt : last;
        {
          const int rem = ntile - nxt;
          if (rem == 2) __builtin_amdgcn_s_setprio(2);
          else if (rem == 1) __builtin_amdgcn_s_setprio(1);
          else if (rem <= 0) __builtin_amdgcn_s_setprio(0);
        }
        const unsigned en = l1[32 * nx];
        PHASE(G1, accA, wA, aA, 1, shi, sB, 128, )
        u32x4 ro[4];
#pragma unroll
        for (int qd = 0; qd < 4; ++qd) ro[qd] = l4[8 * nx + 2 * qd];
        PHASE(G0, accB, wB, aB, 0, slo, sA, 0,
              _Pragma("unroll") for (int qd = 0; qd < 4; ++qd) {
                ad[4 * qd + 0] = (ro[qd].x & 0x7ffe00u) | laneoff; ad[4 * qd + 1] = (ro[qd].y & 0x7ffe00u) | laneoff;
                ad[4 * qd + 2] = (ro[qd].z & 0x7ffe00u) | laneoff; ad[4 * qd + 3] = (ro[qd].w & 0x7ffe00u) | laneoff;
              }
              dn = en & 63u;)
        if (__builtin_expect(!allin, 0)) wait_rows(en);
        PHASE(G1, accB, wB, aB, 1, shi, sA, 128,
              *sp = (unsigned short)0;
              oh = *(const bf16x8*)(lutp + dn * 32);
              ea = *(const bf16x8*)(eab + EA_OFF(en));)
        if (nxt >= ntile) break;
      }
    }
    STAMP(1, 9);
    int after = -1;
    if (mode == 1 && rp + BSLOTS < NP) { rp += BSLOTS; continue; }
    if (mode == 2) {
      if ((rw + 1) * (POOL - 1) < __builtin_amdgcn_readfirstlane((int)((const unsigned*)(ws + WS_SPC))[rp])) { ++rw; continue; }
      after = rp;
    }
    int best = NP;
#pragma unroll
    for (int k = 0; k < 4; ++k) {
      unsigned long long mk = lds.omask[k];
      const int lo = (after + 1 - k + 3) >> 2;
      mk = lo >= 64 ? 0ull : (mk >> lo) << lo;
      if (mk) { const int p = 4 * (int)__builtin_ctzll(mk) + k; best = p < best ? p : best; }
    }
    best = __builtin_amdgcn_readfirstlane(best);
    if (__builtin_expect(best >= NP, 1)) break;
    mode = 2; rp = best; rw = 0;
  }
  STAMP(1, 12);
  {
    const int l2 = launder(tid) & 63;
    float* xch = (float*)lds.ea;
    WAIT_VM(0);
    lds_barrier();
    float* xo = xch + wv * 640 + l2;
#define XROWI(r) (((r) >> 3) * 5 + (((r) & 7) < 4 ? ((r) & 7) : 4))
#pragma unroll
    for (int r = 0; r < 16; ++r) if (crow(r & 7, 0) <= 8) xo[XROWI(r) * 64] = accB[r];
    lds_barrier();
    const float* xi = xch + partner * 640 + l2;
    const int nb = bkt ? 8 : 9;
    const int cc = l2 & 31, hh = l2 >> 5;
    const __amdgpu_buffer_rsrc_t rsO = __builtin_amdgcn_make_buffer_rsrc((void*)out, 0, N_NODES * 256, 0x00020000);
    const unsigned ob = (unsigned)(grp * GN + node0 + 4 * hh) * 256u + (unsigned)(par * 64 + cc) * 2u;
#pragma unroll
    for (int r = 0; r < 16; ++r) {
      const int dl0 = crow(r & 7, 0);
      if (dl0 <= 8) {
        const float z = accA[r] + xi[XROWI(r) * 64];
        const float o = z * __builtin_amdgcn_rcpf(1.f + __builtin_amdgcn_exp2f(z * NLOG2E));
        const unsigned off = ob + (unsigned)dl0 * 256u + (unsigned)(r >> 3) * 64u;
        __builtin_amdgcn_raw_buffer_store_b16((unsigned short)pack_bf16(o, 0.f), rsO, (dl0 < 8 || (hh == 0 && nb == 9)) ? off : OOB, 0, 0);
      }
    }
  }
  STAMP(1, 13);
  STAMP_FLUSH(1);
}

}

extern "C" void kernel_launch(void* const* d_in, const int* in_sizes, int n_in, void* d_out, int out_size,
                              void* d_ws, size_t ws_size, hipStream_t stream) {
  const unsigned short* x = (const unsigned short*)d_in[0];
  const unsigned short* e_attr = (const unsigned short*)d_in[1];
  const int* src = (const int*)d_in[2];
  const int* dst = (const int*)d_in[3];
  const unsigned short* u_w = (const unsigned short*)d_in[4];
  const unsigned short* v_w = (const unsigned short*)d_in[5];
  const unsigned short* g_w = (const unsigned short*)d_in[6];
  k_prep<<<SORT_BLOCKS + GEMM_BLOCKS + WPREP_BLOCKS, 512, 0, stream>>>(x, e_attr, src, dst, u_w, v_w, g_w, (char*)d_ws);
  k_edge<<<NG, NT2, 0, stream>>>(x, e_attr, (const char*)d_ws, (unsigned short*)d_out);
}
```

```cpp
#include <hip/hip_runtime.h>
#include <hip/hip_bf16.h>
#include <stdint.h>

namespace {

constexpr int N_NODES = 12500, N_EDGES = 400000, DM = 128;
constexpr int GN = 49;
constexpr int NG = 256;
constexpr int BPG = 6;
constexpr int NKEY = 12;
constexpr int SRC_HALF = 6250;
constexpr int NP = 256;
constexpr int CH = 1568;
constexpr int FIX = 8;
constexpr int SLOT_REC = 28;
constexpr int BCAP = 384;
constexpr int POOL = 192;
constexpr int BSLOTS = 6;
constexpr int NW = 12;
constexpr int NT2 = NW * 64;
constexpr float NLOG2E = -1.4426950408889634f;
constexpr unsigned OOB = 0x80000000u;

constexpr size_t TAB_BYTES = (size_t)N_NODES * 512;
constexpr size_t WS_G = 0;
constexpr size_t WS_WF = 6400000;
constexpr size_t WS_SPC = WS_WF + (576 + 4096) * 16;
constexpr size_t WS_SL = WS_SPC + 1024;
constexpr size_t WS_XR = WS_SL + (size_t)NP * NG * 64;
constexpr size_t WS_SP = WS_XR + (size_t)NP * NG * 192;
static_assert(WS_SL % 256 == 0, "alignment");

typedef short bf16x8 __attribute__((ext_vector_type(8)));
typedef short s16x4 __attribute__((ext_vector_type(4)));
typedef float f32x16 __attribute__((ext_vector_type(16)));
typedef float f32x2 __attribute__((ext_vector_type(2)));
typedef __bf16 bf16x2_t __attribute__((ext_vector_type(2)));
typedef unsigned u32x4 __attribute__((ext_vector_type(4)));
typedef unsigned u32x2 __attribute__((ext_vector_type(2)));
typedef __attribute__((address_space(3))) s16x4* lds_s16x4_ptr;
typedef __attribute__((address_space(3))) void* lds_ptr;

#define STAMP_DECL
#define STAMP_FLUSH(k) do {} while (0)
#define STAMP(k, i) do {} while (0)
#define NSTAMP(n) 0
#define GLOAD(a) __uint_as_float(__builtin_amdgcn_raw_buffer_load_b32(rsG, (a), 0, 0))
#define EXP2(x) __builtin_amdgcn_exp2f(x)
#define RCP(x) __builtin_amdgcn_rcpf(x)
#define MFMA32(a, b, c) __builtin_amdgcn_mfma_f32_32x32x16_bf16((a), (b), (c), 0, 0, 0)
#define DMA16(rs, ldsp, vo, so)                                                                               \
  asm volatile("s_mov_b32 m0, %0\n\ts_nop 0\n\tbuffer_load_dwordx4 %1, %2, %3 offen lds"                   \
               :: "s"((unsigned)(uintptr_t)(lds_ptr)(ldsp)), "v"((unsigned)(vo)), "s"(rs), "s"((unsigned)(so)) : "memory")
#define DMA16R(rs, ldsa, vo, so)                                                                              \
  asm volatile("s_mov_b32 m0, %0\n\ts_nop 0\n\tbuffer_load_dwordx4 %1, %2, %3 offen lds"                   \
               :: "s"((unsigned)(ldsa)), "v"((unsigned)(vo)), "s"(rs), "s"((unsigned)(so)) : "memory")
#define WAIT_VM(n) asm volatile("s_waitcnt vmcnt(%0)" :: "n"(n) : "memory")

__device__ __forceinline__ unsigned pack_bf16(float lo, float hi) {
  f32x2 v; v.x = lo; v.y = hi;
  bf16x2_t r = __builtin_convertvector(v, bf16x2_t);
  return __builtin_bit_cast(unsigned, r);
}
__device__ __forceinline__ u32x4 make_srd(const void* base, unsigned bytes) {
  const uintptr_t a = (uintptr_t)base;
  return u32x4{(unsigned)a, (unsigned)(a >> 32) & 0xffffu, bytes, 0x00020000u};
}
__device__ __forceinline__ int crow(int reg, int h) { return (reg & 3) + 8 * (reg >> 2) + 4 * h; }
__device__ __forceinline__ int launder(int v) { asm volatile("" : "+v"(v)); return v; }
__device__ __forceinline__ void lds_barrier() { asm volatile("s_waitcnt lgkmcnt(0)\n\ts_barrier" ::: "memory"); }

__device__ __forceinline__ void wait_flag_mask(unsigned* flag, unsigned mask, unsigned n) {
  const unsigned a = (unsigned)(uintptr_t)(lds_ptr)flag;
  for (;;) {
    unsigned v;
    asm volatile("ds_read_b32 %0, %1\n\ts_waitcnt lgkmcnt(0)" : "=v"(v) : "v"(a) : "memory");
    if (((unsigned)__builtin_amdgcn_readfirstlane((int)v) & mask) >= n) break;
    __builtin_amdgcn_s_sleep(1);
  }
}
__device__ __forceinline__ void wait_flag(unsigned* flag, unsigned n) { wait_flag_mask(flag, ~0u, n); }

__device__ __forceinline__ int wave_incl_scan(int x) {
  x += __builtin_amdgcn_update_dpp(0, x, 0x111, 0xf, 0xf, false);
  x += __builtin_amdgcn_update_dpp(0, x, 0x112, 0xf, 0xf, false);
  x += __builtin_amdgcn_update_dpp(0, x, 0x114, 0xf, 0xf, false);
  x += __builtin_amdgcn_update_dpp(0, x, 0x118, 0xf, 0xf, false);
  x += __builtin_amdgcn_update_dpp(0, x, 0x142, 0xa, 0xf, false);
  x += __builtin_amdgcn_update_dpp(0, x, 0x143, 0xc, 0xf, false);
  return x;
}
__device__ __forceinline__ unsigned xr_index(unsigned s, unsigned k) { return k < 8u ? s * 8u + k : (unsigned)(NP * NG * 8) + s * 16u + (k - 8u); }
__device__ __forceinline__ unsigned local_bits(unsigned r) { return ((r & 7u) << 6) | ((r >> 3) << 23); }
__device__ __forceinline__ unsigned rec_eoff(unsigned rec, unsigned pbase) {
  return (pbase + (((rec >> 20) & 0x7f8u) | ((rec >> 6) & 7u))) * 32u;
}

constexpr int SORT_BLOCKS = NP;
constexpr int GEMM_BLOCKS = 208;
constexpr int WPREP_BLOCKS = 9;

struct SortLds {
  unsigned kc[NG * NKEY / 2];
  unsigned gc[NG];
  unsigned img[NG * 16];
  unsigned spc;
};
struct GemmLds {
  unsigned short w[2][2][128][32];
  unsigned char xt[128 * 272];
};
union PrepLds { SortLds s; GemmLds g; };
static_assert(sizeof(PrepLds) <= 81920, "two workgroups per CU");

__global__ __launch_bounds__(512, 4) void k_prep(const unsigned short* __restrict__ x, const unsigned short* __restrict__ e_attr,
                                                 const int* __restrict__ src, const int* __restrict__ dst,
                                                 const unsigned short* __restrict__ u_w, const unsigned short* __restrict__ v_w,
                                                 const unsigned short* __restrict__ g_w, char* __restrict__ ws) {
  __shared__ __attribute__((aligned(16))) PrepLds lds;
  const int tid = threadIdx.x, lane = tid & 63;
  const int wv = __builtin_amdgcn_readfirstlane(tid >> 6);
  const int blk = blockIdx.x;
  STAMP_DECL
  STAMP(0, 0);
  if (blk < SORT_BLOCKS) {
    const int pidx = blk;
    const int nedge = N_EDGES - pidx * CH < CH ? N_EDGES - pidx * CH : CH;
    const bool have = tid * 4 < nedge;
    int4 dv = make_int4(0, 0, 0, 0), sv = make_int4(0, 0, 0, 0);
    if (have) { dv = *(const int4*)(dst + pidx * CH + tid * 4); sv = *(const int4*)(src + pidx * CH + tid * 4); }
    if (tid < 448) ((u32x4*)lds.s.kc)[tid] = u32x4{0, 0, 0, 0};
    if (tid == 511) lds.s.spc = 0u;
    lds_barrier();
    STAMP(0, 1);
    if (have) {
      const int d4[4] = {dv.x, dv.y, dv.z, dv.w}, s4[4] = {sv.x, sv.y, sv.z, sv.w};
      unsigned g[4], dn[4], sc[4], idx[4];
#pragma unroll
      for (int k = 0; k < 4; ++k) {
        unsigned d = (unsigned)d4[k]; d = d < (unsigned)N_NODES ? d : (unsigned)(N_NODES - 1);
        sc[k] = (unsigned)s4[k]; sc[k] = sc[k] < (unsigned)N_NODES ? sc[k] : (unsigned)(N_NODES - 1);
        g[k] = d / GN; dn[k] = d - g[k] * GN;
        idx[k] = atomicAdd(&lds.s.gc[g[k]], 1u);
      }
      unsigned rk[4], k12[4];
#pragma unroll
      for (int k = 0; k < 4; ++k) {
        const unsigned b = dn[k] == 0 ? 0u : (dn[k] - 1) >> 3;
        const unsigned sh = sc[k] >= (unsigned)SRC_HALF ? 16u : 0u;
        k12[k] = b * 2 + (sh >> 4);
        rk[k] = (atomicAdd(&lds.s.kc[g[k] * 6 + b], idx[k] < (unsigned)SLOT_REC ? 1u << sh : 0u) >> sh) & 0xffffu;
      }
#pragma unroll
      for (int k = 0; k < 4; ++k) {
        const unsigned rec = (sc[k] << 9) | dn[k] | local_bits((unsigned)(tid * 4 + k));
        if (idx[k] < (unsigned)FIX) {
          lds.s.img[g[k] * 16 + 4 + idx[k]] = rec;
          ((unsigned short*)lds.s.img)[g[k] * 32 + 24 + idx[k]] = (unsigned short)(k12[k] | (rk[k] << 4));
        } else if (idx[k] < (unsigned)SLOT_REC) {
          ((u32x2*)(ws + WS_XR))[xr_index((unsigned)pidx * NG + g[k], idx[k] - FIX)] = u32x2{rec, k12[k] | (rk[k] << 4)};
        } else {
          const unsigned sp = atomicAdd(&lds.s.spc, 1u);
          ((u32x2*)(ws + WS_SP))[(size_t)pidx * CH + sp] = u32x2{rec, g[k]};
        }
      }
    }
    STAMP(0, 2);
    STAMP(0, 3);
    lds_barrier();
    STAMP(0, 4);
    if (tid < NG) {
      unsigned hd[2] = {0u, 0u};
#pragma unroll
      for (int i = 0; i < 6; ++i) {
        const unsigned cc = lds.s.kc[tid * 6 + i];
        hd[i / 3] |= ((cc & 31u) | ((cc >> 16) << 5)) << (10 * (i % 3));
      }
      ((u32x4*)lds.s.img)[tid * 4] = u32x4{hd[0], hd[1], lds.s.gc[tid], 0u};
      if (tid == 0) ((unsigned*)(ws + WS_SPC))[pidx] = lds.s.spc;
    }
    lds_barrier();
    {
      u32x4* so = (u32x4*)(ws + WS_SL) + (size_t)pidx * NG * 4;
      so[tid] = ((const u32x4*)lds.s.img)[tid];
      so[tid + 512] = ((const u32x4*)lds.s.img)[tid + 512];
    }
    STAMP(0, 8);
    STAMP_FLUSH(0);
  } else if (blk < SORT_BLOCKS + GEMM_BLOCKS) {
    const int gb = blk - SORT_BLOCKS;
    const int j = gb >> 3;
    const int rg = (gb & 7) + 8 * (j >> 1), cp = j & 1;
    if (rg * 128 >= N_NODES) return;
    const int m = lane & 31, h = lane >> 5;
    const int cbl = wv & 1, cb = cp * 2 + cbl;
    const int rbl = wv >> 1;
    const int row0 = rg * 128 + rbl * 32;
    u32x4 wld[4], xld[4];
#pragma unroll
    for (int i = 0; i < 4; ++i) {
      const int q = tid + 512 * i;
      int xr = rg * 128 + (q >> 4); xr = xr < N_NODES ? xr : N_NODES - 1;
      xld[i] = *(const u32x4*)(x + (size_t)xr * DM + (q & 15) * 8);
    }
#pragma unroll
    for (int i = 0; i < 4; ++i) {
      const int q = tid + 512 * (i & 1);
      const unsigned short* W = (i >> 1) == 0 ? g_w + 128 * DM : v_w;
      wld[i] = *(const u32x4*)(W + (size_t)(q >> 3) * DM + cp * 64 + (q & 7) * 8);
    }
    __builtin_amdgcn_sched_barrier(0);
#pragma unroll
    for (int i = 0; i < 4; ++i) {
      const int q = tid + 512 * i;
      *(u32x4*)(lds.g.xt + (q >> 4) * 272 + (q & 15) * 16) = xld[i];
    }
#pragma unroll
    for (int i = 0; i < 4; ++i) {
      const int q = tid + 512 * (i & 1);
      *(u32x4*)&lds.g.w[i >> 1][(q >> 2) & 1][q >> 3][(q & 3) * 8] = wld[i];
    }
    STAMP(0, 1);
    lds_barrier();
    STAMP(0, 2);
    bf16x8 xa[8];
#pragma unroll
    for (int s = 0; s < 8; ++s) xa[s] = *(const bf16x8*)(lds.g.xt + (rbl * 32 + m) * 272 + s * 32 + h * 16);
    const int q4 = (lane & 15) >> 2, p4 = lane & 3, blk16 = (lane >> 4) & 1;
    const unsigned goff = (unsigned)(row0 + 4 * h) * 512u + (unsigned)(cb * 32 + m) * 4u;
    {
      f32x16 acc0, acc1;
#pragma unroll
      for (int i = 0; i < 16; ++i) { acc0[i] = 0.f; acc1[i] = 0.f; }
#pragma unroll
      for (int s = 0; s < 8; ++s) {
        const int kr = 16 * s + 8 * h + q4;
        s16x4 b0 = __builtin_amdgcn_ds_read_tr16_b64_v4i16((lds_s16x4_ptr)&lds.g.w[0][cbl][kr][blk16 * 16 + p4 * 4]);
        s16x4 b1 = __builtin_amdgcn_ds_read_tr16_b64_v4i16((lds_s16x4_ptr)&lds.g.w[0][cbl][kr + 4][blk16 * 16 + p4 * 4]);
        s16x4 v0 = __builtin_amdgcn_ds_read_tr16_b64_v4i16((lds_s16x4_ptr)&lds.g.w[1][cbl][kr][blk16 * 16 + p4 * 4]);
        s16x4 v1 = __builtin_amdgcn_ds_read_tr16_b64_v4i16((lds_s16x4_ptr)&lds.g.w[1][cbl][kr + 4][blk16 * 16 + p4 * 4]);
        acc0 = MFMA32(xa[s], __builtin_shufflevector(b0, b1, 0, 1, 2, 3, 4, 5, 6, 7), acc0);
        acc1 = MFMA32(xa[s], __builtin_shufflevector(v0, v1, 0, 1, 2, 3, 4, 5, 6, 7), acc1);
      }
      STAMP(0, 3);
      const __amdgpu_buffer_rsrc_t rsG = __builtin_amdgcn_make_buffer_rsrc((void*)(ws + WS_G), 0, (int)TAB_BYTES, 0x00020000);
#pragma unroll
      for (int r = 0; r < 16; r += 2) {
        const unsigned hvp = pack_bf16(acc1[r], acc1[r + 1]);
        float i0 = __builtin_amdgcn_rcpf(__uint_as_float(hvp << 16));
        float i1 = __builtin_amdgcn_rcpf(__uint_as_float(hvp & 0xffff0000u));
        i0 = __builtin_amdgcn_fmed3f(i0, -3.3895313892515355e38f, 3.3895313892515355e38f);
        i1 = __builtin_amdgcn_fmed3f(i1, -3.3895313892515355e38f, 3.3895313892515355e38f);
        const unsigned ip = pack_bf16(i0, i1);
#pragma unroll
        for (int k = 0; k < 2; ++k) {
          const unsigned L = k ? ip >> 16 : ip & 0xffffu;
          const float bsc = acc0[r + k] * NLOG2E;
          const unsigned mg = __float_as_uint(__builtin_amdgcn_fmed3f(__builtin_fabsf(bsc), 1.17549435e-38f, 3.37e38f));
          const unsigned t = mg + 0x8000u - L;
          const unsigned sl = (__float_as_uint(bsc) & 0x80000000u) | L;
          __builtin_amdgcn_raw_buffer_store_b32((t & 0x7fff0000u) | sl, rsG, goff + (unsigned)crow(r + k, 0) * 512u, 0, 0);
        }
      }
    }
    STAMP(0, 4);
    STAMP_FLUSH(0);
  } else {
    u32x4* wf = (u32x4*)(ws + WS_WF);
    const int l = tid & 63;
    const int wb = blk - SORT_BLOCKS - GEMM_BLOCKS;
    if (wb > 0) {
      const int idx = (wb - 1) * 512 + tid;
      const int ks = (idx >> 6) & 7, cb = (idx >> 9) & 3;
      const unsigned short* W = (idx >> 11) ? u_w : g_w;
      unsigned v[8];
#pragma unroll
      for (int j = 0; j < 8; ++j) v[j] = W[(size_t)(16 * ks + 8 * (l >> 5) + j) * DM + cb * 32 + (l & 31)];
      wf[576 + idx] = u32x4{v[0] | (v[1] << 16), v[2] | (v[3] << 16), v[4] | (v[5] << 16), v[6] | (v[7] << 16)};
    } else if (tid < 256) {
      const int cb = tid >> 6;
      unsigned hi[4], lo[4];
#pragma unroll
      for (int jj = 0; jj < 4; ++jj) {
        const float w0 = __uint_as_float((unsigned)g_w[(size_t)(256 + 8 * (l >> 5) + 2 * jj) * DM + cb * 32 + (l & 31)] << 16) * NLOG2E;
        const float w1 = __uint_as_float((unsigned)g_w[(size_t)(256 + 8 * (l >> 5) + 2 * jj + 1) * DM + cb * 32 + (l & 31)] << 16) * NLOG2E;
        hi[jj] = pack_bf16(w0, w1);
        lo[jj] = pack_bf16(w0 - __uint_as_float(hi[jj] << 16), w1 - __uint_as_float(hi[jj] & 0xffff0000u));
      }
      wf[(cb * 2 + 0) * 64 + l] = u32x4{hi[0], hi[1], hi[2], hi[3]};
      wf[(cb * 2 + 1) * 64 + l] = u32x4{lo[0], lo[1], lo[2], lo[3]};
    } else if (tid < 320) {
      const int dl = l >> 1, hh = l & 1;
      u32x4 v = u32x4{0, 0, 0, 0};
      if (dl < 16 && ((dl >> 2) & 1) == hh) {
        const int j = ((dl >> 3) << 2) | (dl & 3);
        const unsigned one = (j & 1) ? 0x3f800000u : 0x00003f80u;
        v.x = (j >> 1) == 0 ? one : 0u; v.y = (j >> 1) == 1 ? one : 0u; v.z = (j >> 1) == 2 ? one : 0u; v.w = (j >> 1) == 3 ? one : 0u;
      }
      wf[512 + l] = v;
    }
  }
}

constexpr int SROW = 80;
constexpr int SMAT = 17 * SROW + 48;
constexpr int LISTN = BPG * BCAP;
#define GPF_ROWS 196
#define LDEPTH 4
constexpr int RROW = 272;
struct EdgeLds {
  u32x4 ea[(NP * FIX + POOL) * 2];
  u32x4 wop[8 * 64];
  u32x4 sl[NP * 4];
  unsigned char hu[58 * RROW];
  unsigned short adj[NP * NKEY];
  unsigned char ap[58 * RROW];
  u32x4 lut[64];
  unsigned list[LISTN + 32];
  unsigned char smat[NW * SMAT];
  unsigned tot[8];
  unsigned long long omask[4];
  unsigned short xdesc[256];
  unsigned tick[8];
  unsigned slow, sdone, bdone, ready;
  unsigned eaprog;
};
static_assert(sizeof(EdgeLds) <= 163840 - 1536, "LDS");
static_assert(NW * SMAT >= 13 * 1024, "x tile alias");

__global__ __launch_bounds__(NT2) void k_edge(const unsigned short* __restrict__ x, const unsigned short* __restrict__ e_attr,
                                              const char* __restrict__ ws, unsigned short* __restrict__ out) {
  __shared__ __attribute__((aligned(16))) EdgeLds lds;
  const int tid = threadIdx.x, lane = tid & 63;
  const int wv = __builtin_amdgcn_readfirstlane(tid >> 6);
  const int grp = (blockIdx.x & 7) * 32 + (blockIdx.x >> 3);
  const int bkt = (int)((0x545432231100ull >> (4 * wv)) & 15), par = (int)((0xc56u >> wv) & 1) ;
  const int partner = (int)((0x98ba45672301ull >> (4 * wv)) & 15);
  const int c = lane & 31, h = lane >> 5;
  STAMP_DECL
  STAMP(1, 0);
  const u32x4 rsS = make_srd(ws + WS_SL, NP * NG * 64);
  const u32x4 rsX = make_srd(x, N_NODES * 256);
  const u32x4 rsL = make_srd(ws + WS_WF, 576 * 16);
  const __amdgpu_buffer_rsrc_t rsW = __builtin_amdgcn_make_buffer_rsrc((void*)(ws + WS_WF), 0, (576 + 4096) * 16, 0x00020000);

  if (tid == 0) { lds.sdone = 0u; lds.bdone = 0u; lds.ready = 0u; lds.eaprog = 0u; }
  lds_barrier();
  const unsigned w = (unsigned)wv;
  unsigned char* smat = lds.smat + wv * SMAT;
  if (wv == 9) {
    const unsigned l_w = (unsigned)(uintptr_t)(lds_ptr)lds.wop, l_lut = (unsigned)(uintptr_t)(lds_ptr)lds.lut;
#pragma unroll
    for (int i = 0; i < 8; ++i) DMA16R(rsL, l_w + i * 1024u, (unsigned)lane * 16u, i * 1024u);
    DMA16R(rsL, l_lut, (unsigned)lane * 16u, 512u * 16u);
    WAIT_VM(0);
    if (lane == 0) atomicAdd(&lds.ready, 1u);
  }
  const u32x4 rsEs = make_srd(e_attr, N_EDGES * 32);
  if (wv >= 9) {
    const unsigned l_ea = (unsigned)(uintptr_t)(lds_ptr)lds.ea;
    const unsigned idx = ((unsigned)lane >> 1) & 7u;
    const unsigned* sp = (const unsigned*)lds.sl + ((w - 9u) * 4u + ((unsigned)lane >> 4)) * 16u;
    unsigned pb = ((w - 9u) * 4u + ((unsigned)lane >> 4)) * CH * 32u + ((unsigned)lane & 1u) * 16u;
    wait_flag_mask(&lds.sdone, 0x10000u, 0x10000u);
    STAMP(1, 2);
    unsigned char* prog = (unsigned char*)&lds.eaprog + (w - 9u);
#pragma unroll 1
    for (int i = 0; i < 22; i += 2) {
#pragma unroll
      for (int u = 0; u < 2; ++u) {
        unsigned n = (w - 9u) + 3u * (i + u);
        const bool in = n < 64u;
        const unsigned rec = sp[in ? (i + u) * 192 + 4 + idx : 0], total = in ? sp[(i + u) * 192 + 2] : 0u;
        const unsigned vo = idx < total ? pb + (((rec >> 20) & 0x7f8u) | ((rec >> 6) & 7u)) * 32u : OOB;
        if (in) DMA16R(rsEs, l_ea + n * 1024u, vo, 0u);
        pb += 12u * CH * 32u;
      }
      if (i >= LDEPTH && i < 20) { WAIT_VM(LDEPTH); if (lane == 0) *prog = (unsigned char)(i + 2 - LDEPTH); }
    }
    WAIT_VM(0); if (lane == 0) *prog = (unsigned char)22;
    STAMP(1, 4);
    wait_flag_mask(&lds.bdone, 255u, BPG + 1);
  }
  asm volatile("" ::: "memory");
  if (wv == 8) {
    if (lane < 2) lds.ea[NP * FIX * 2 + lane] = u32x4{0, 0, 0, 0};
    wait_flag_mask(&lds.sdone, 255u, 8);
    STAMP(1, 2);
    int xn[4], sum = 0;
    bool big = false;
#pragma unroll
    for (int k = 0; k < 4; ++k) {
      const unsigned total = ((const unsigned*)lds.sl)[(lane * 4 + k) * 16 + 2];
      const unsigned tc = total < (unsigned)SLOT_REC ? total : (unsigned)SLOT_REC;
      xn[k] = tc > (unsigned)FIX ? (int)tc - FIX : 0;
      sum += xn[k];
      big |= total > (unsigned)SLOT_REC;
    }
    const int inc = wave_incl_scan(sum);
    int npool = __builtin_amdgcn_readlane(inc, 63);
    const bool slow = npool > POOL - 1;
    if (slow) npool = 0;
    if (npool > 0) {
      const int p0 = inc - sum, p1 = p0 + xn[0], p2 = p1 + xn[1], p3 = p2 + xn[2];
#pragma unroll 1
      for (int i = 0; __any(i < xn[0] || i < xn[1] || i < xn[2] || i < xn[3]); ++i) {
        const int d = (lane << 7) | i;
        if (i < xn[0]) lds.xdesc[p0 + i] = (unsigned short)d;
        if (i < xn[1]) lds.xdesc[p1 + i] = (unsigned short)(d + 32);
        if (i < xn[2]) lds.xdesc[p2 + i] = (unsigned short)(d + 64);
        if (i < xn[3]) lds.xdesc[p3 + i] = (unsigned short)(d + 96);
      }
    }
    const __amdgpu_buffer_rsrc_t rsO = __builtin_amdgcn_make_buffer_rsrc((void*)(ws + WS_XR), 0, NP * NG * 192, 0x00020000);
    u32x2 xrec[3]; unsigned xd[3];
#pragma unroll
    for (int r = 0; r < 3; ++r) {
      xd[r] = lds.xdesc[lane + 64 * r];
      const unsigned e = xr_index((xd[r] >> 5) * NG + (unsigned)grp, xd[r] & 31u);
      xrec[r] = __builtin_amdgcn_raw_buffer_load_b64(rsO, lane + 64 * r < npool ? e * 8u : OOB, 0, 0);
    }
    if (lane == 0) atomicAdd(&lds.sdone, 0x10000u);
    if (lane < 4) lds.omask[lane] = 0ull;
    if (__builtin_expect(__any(big), 0)) {
#pragma unroll
      for (int k = 0; k < 4; ++k) {
        const unsigned long long mk = __ballot(((const unsigned*)lds.sl)[(lane * 4 + k) * 16 + 2] > (unsigned)SLOT_REC);
        if (lane == 0) lds.omask[k] = mk;
      }
    }
    if (lane == 0) lds.slow = slow ? 1u : 0u;
    asm volatile("s_waitcnt lgkmcnt(0)" ::: "memory");
    if (lane == 0) atomicAdd(&lds.bdone, 1u);
    STAMP(1, 4);
    {
      const unsigned l_p = (unsigned)(uintptr_t)(lds_ptr)&lds.ea[(NP * FIX + 1) * 2];
#pragma unroll
      for (int r = 0; r < 3; ++r) {
        const unsigned eo = lane + 64 * r < npool ? rec_eoff(xrec[r].x, (xd[r] >> 5) * CH) : OOB;
#pragma unroll
        for (int q = 0; q < 2; ++q) {
          const unsigned v = (unsigned)__builtin_amdgcn_ds_bpermute((q * 32 + (lane >> 1)) * 4, (int)eo) + ((unsigned)lane & 1u) * 16u;
          if (64 * r + 32 * q < npool && (64 * r + 32 * q + 32 < POOL || lane < 62)) DMA16R(rsEs, l_p + (2 * r + q) * 1024u, v, 0u);
        }
      }
    }
    wait_flag_mask(&lds.bdone, 255u, BPG + 1);
    STAMP(1, 5);
#pragma unroll
    for (int r = 0; r < 3; ++r) {
      if (lane + 64 * r < npool) {
        const int pos = lds.adj[(xd[r] >> 5) * NKEY + (xrec[r].y & 15)] + (int)(xrec[r].y >> 4);
        if (pos < LISTN) lds.list[pos] = (xrec[r].x & 0x7ffe3fu) | local_bits((unsigned)(NP * FIX + 1 + lane + 64 * r));
      }
    }
    asm volatile("s_waitcnt lgkmcnt(0)" ::: "memory");
    if (lane == 0) atomicAdd(&lds.ready, 1u);
    WAIT_VM(0);
    if (lane == 0) ((unsigned char*)&lds.eaprog)[3] = (unsigned char)1;
  }
  asm volatile("" ::: "memory");
  if (wv < 8) {
    {
      const unsigned vl = (unsigned)lane * 16u;
      const unsigned vs = (unsigned)(lane >> 2) * (NG * 64) + (unsigned)(lane & 3) * 16u;
      const unsigned l_sl = (unsigned)(uintptr_t)(lds_ptr)lds.sl, l_xt = (unsigned)(uintptr_t)(lds_ptr)lds.smat;
      const unsigned xb = (unsigned)grp * (GN * 256);
      DMA16R(rsS, l_sl + w * 2048u, vs, (w * 32u * NG + (unsigned)grp) * 64u);
      DMA16R(rsS, l_sl + w * 2048u + 1024u, vs, ((w * 32u + 16u) * NG + (unsigned)grp) * 64u);
      const unsigned vx0 = (vl & 0x300u) | ((((unsigned)lane & 15u) ^ ((4u * w + ((unsigned)lane >> 4)) & 15u)) << 4);
      DMA16R(rsX, l_xt + w * 1024u, vx0, xb + w * 1024u);
      const unsigned n2 = wv < 5 ? w + 8u : w;
      DMA16R(rsX, l_xt + n2 * 1024u, vx0, xb + n2 * 1024u);
    }
    WAIT_VM(2);
    if (lane == 0) atomicAdd(&lds.sdone, 1u);
    bf16x8 wq[8];
#pragma unroll
    for (int ks = 0; ks < 8; ++ks)
      wq[ks] = __builtin_bit_cast(bf16x8, __builtin_amdgcn_raw_buffer_load_b128(rsW, (unsigned)lane * 16u, (576 + (wv * 8 + ks) * 64) * 16, 0));
    STAMP(1, 3);
    WAIT_VM(8);
    if (lane == 0) atomicAdd(&lds.sdone, 256u);
    STAMP(1, 1);
    wait_flag_mask(&lds.sdone, 255u, 8);
    STAMP(1, 2);
    if (wv < BPG) {
      unsigned cn[4], sum = 0;
#pragma unroll
      for (int k = 0; k < 4; ++k) {
        const unsigned f = ((const unsigned*)lds.sl)[(lane * 4 + k) * 16 + (wv >= 3)] >> (10 * (wv % 3));
        cn[k] = (f & 31u) | (((f >> 5) & 31u) << 16);
        sum += cn[k];
      }
      const unsigned inc = (unsigned)wave_incl_scan((int)sum);
      const unsigned totp = (unsigned)__builtin_amdgcn_readlane((int)inc, 63);
      unsigned run = inc - sum + ((unsigned)(wv * BCAP) | (((unsigned)(wv * BCAP) + (totp & 0xffffu)) << 16));
#pragma unroll
      for (int k = 0; k < 4; ++k) { ((unsigned*)lds.adj)[(lane * 4 + k) * 6 + wv] = run; run += cn[k]; }
      {
        const int tb = (int)((totp & 0xffffu) + (totp >> 16));
        if (lane == 0) { lds.tot[wv] = (unsigned)tb; lds.tick[wv] = 0u; }
        const int e = tb + c;
        if (e < ((tb + 31) & ~31) && e < BCAP) lds.list[wv * BCAP + e] = (unsigned)((wv ? 8 * wv + 1 : 0) + 16) | local_bits(NP * FIX);
      }
      asm volatile("s_waitcnt lgkmcnt(0)" ::: "memory");
      if (lane == 0) atomicAdd(&lds.bdone, 1u + 256u);
    }
    STAMP(1, 4);
    wait_flag(&lds.bdone, BPG * 256);
    STAMP(1, 5);
    {
      const int p = tid >> 1, j = tid & 1;
      const u32x4 rc = lds.sl[p * 4 + 1 + j];
      const u32x2 kr = ((const u32x2*)lds.sl)[p * 8 + 6 + j];
      const int total = (int)((const unsigned*)lds.sl)[p * 16 + 2] - 4 * j;
      const unsigned short* ar = &lds.adj[p * NKEY];
      int a0 = ar[kr.x & 15] + ((kr.x >> 4) & 31), a1 = ar[(kr.x >> 16) & 15] + ((kr.x >> 20) & 31);
      int a2 = ar[kr.y & 15] + ((kr.y >> 4) & 31), a3 = ar[(kr.y >> 16) & 15] + ((kr.y >> 20) & 31);
      a0 = (total > 0 && a0 < LISTN) ? a0 : LISTN + c; a1 = (total > 1 && a1 < LISTN) ? a1 : LISTN + c;
      a2 = (total > 2 && a2 < LISTN) ? a2 : LISTN + c; a3 = (total > 3 && a3 < LISTN) ? a3 : LISTN + c;
      const unsigned eb = ((unsigned)p << 23) | ((unsigned)j << 8);
      lds.list[a0] = (rc.x & 0x7ffe3fu) | eb;
      lds.list[a1] = (rc.y & 0x7ffe3fu) | (eb + 64u);
      lds.list[a2] = (rc.z & 0x7ffe3fu) | (eb + 128u);
      lds.list[a3] = (rc.w & 0x7ffe3fu) | (eb + 192u);
    }
    wait_flag_mask(&lds.sdone, 0xff00u, 8 * 256);
    const float sc = wv < 4 ? NLOG2E : 1.0f;
    unsigned char* dp = (wv < 4 ? lds.ap : lds.hu) + (4 * h) * RROW + ((wv & 3) * 32 + c) * 2;
#pragma unroll
    for (int rb = 0; rb < 2; ++rb) {
      int rr = rb * 32 + c; rr = rr < GN ? rr : GN - 1;
      const char* xa = (const char*)lds.smat + rr * 256;
      const int sw16 = (rr & 15) * 16;
      f32x16 ac;
#pragma unroll
      for (int i = 0; i < 16; ++i) ac[i] = 0.f;
#pragma unroll
      for (int ks = 0; ks < 8; ++ks) ac = MFMA32(*(const bf16x8*)(xa + ((ks * 32 + h * 16) ^ sw16)), wq[ks], ac);
#pragma unroll
      for (int r = 0; r < 16; r += 2) {
        const unsigned pk = pack_bf16(ac[r] * sc, ac[r + 1] * sc);
        if (rb * 32 + crow(r, 0) < 56 || (rb * 32 + crow(r, 0) == 56 && h == 0)) {
          *(unsigned short*)(dp + (rb * 32 + crow(r, 0)) * RROW) = (unsigned short)pk;
          *(unsigned short*)(dp + (rb * 32 + crow(r, 0) + 1) * RROW) = (unsigned short)(pk >> 16);
        }
      }
    }
    asm volatile("s_waitcnt lgkmcnt(0)" ::: "memory");
    if (lane == 0) atomicAdd(&lds.ready, 1u);
    wait_flag_mask(&lds.bdone, 255u, BPG + 1);
  }
  STAMP(1, 6);
  bool batch;
  {
    int maxtot = 0;
#pragma unroll
    for (int b = 0; b < BPG; ++b) { const int t = (int)lds.tot[b]; maxtot = t > maxtot ? t : maxtot; }
    batch = __builtin_amdgcn_readfirstlane((int)(maxtot > BCAP || lds.slow != 0u)) != 0;
  }

  const __amdgpu_buffer_rsrc_t rsG = __builtin_amdgcn_make_buffer_rsrc((void*)(ws + WS_G), 0, (int)TAB_BYTES, 0x00020000);
  const unsigned char* eab = (const unsigned char*)lds.ea + h * 16;
#define EA_OFF(e) ((((e) >> 15) & 0x1ff00u) | (((e) >> 1) & 0xe0u))
  const int node0 = bkt ? 8 * bkt + 1 : 0;
  const unsigned laneoff = (unsigned)(c * 4);
  const int sA = par * 256, sB = 256 - sA;
  const unsigned spos = (unsigned)((c >> 4) * 32 + ((c >> 2) & 1) * 16 + ((((c >> 3) & 1) << 2) | (c & 3)) * 2);
  unsigned char* sw = smat + spos - node0 * SROW;
  const unsigned char* lutp = (const unsigned char*)lds.lut + h * 16 - node0 * 32;
  const unsigned char* srow = smat + (c & 15) * SROW + h * 16;
  const unsigned char* zr = (const unsigned char*)&lds.lut[40] + h * 16;
  const unsigned char* slo = c < 16 ? srow : zr;
  const unsigned char* shi = c < 16 ? zr : srow;
  const unsigned char* wA = (const unsigned char*)lds.wop + lane * 16 + par * 4096;
  const unsigned char* wB = (const unsigned char*)lds.wop + lane * 16 + (1 - par) * 4096;
  const unsigned char* ab0 = lds.ap + (node0 + 4 * h + ((lane & 15) >> 2)) * RROW + (((lane >> 4) & 1) * 16 + (lane & 3) * 4) * 2;
  const unsigned char* aA = ab0 + par * 128;
  const unsigned char* aB = ab0 + (1 - par) * 128;
  f32x16 accA, accB;

  int mode = batch ? 1 : 0, rp = 0, rw = 0;
  bool first = true, allin = false;
  auto wait_rows = [&](unsigned e) __attribute__((always_inline)) {
    const unsigned a = (unsigned)(uintptr_t)(lds_ptr)&lds.eaprog;
    for (;;) {
      unsigned v;
      asm volatile("ds_read_b32 %0, %1\n\ts_waitcnt lgkmcnt(0)" : "=v"(v) : "v"(a) : "memory");
      const unsigned pv = (unsigned)__builtin_amdgcn_readfirstlane((int)v);
      if (pv == 0x01161616u) { allin = true; break; }
      unsigned m = pv & 255u, m1 = (pv >> 8) & 255u, m2 = (pv >> 16) & 255u;
      m = m < m1 ? m : m1; m = m < m2 ? m : m2;
      const unsigned hi = e >> 23;
      const bool pad = (e & 0xff8001c0u) == 0x80000000u;
      if (!__any(hi < 256u ? hi >= 12u * m : (!(pv >> 24) && !pad))) break;
      __builtin_amdgcn_s_sleep(2);
    }
  };
  for (;;) {
    const int tl = launder(tid);
    if (__builtin_expect(mode != 0, 0)) {
      WAIT_VM(0);
      __syncthreads();
      if (tl < 8) lds.tot[tl] = 0;
      __syncthreads();
      bool ok = false; unsigned rec = 0, pb = 0;
      if (mode == 1) {
        const int p = rp + tl / SLOT_REC, idx = tl % SLOT_REC;
        if (tl < BSLOTS * SLOT_REC && p < NP && (unsigned)idx < lds.sl[p * 4].z) {
          ok = true; pb = (unsigned)p * CH;
          rec = idx < FIX ? ((const unsigned*)lds.sl)[p * 16 + 4 + idx] : ((const u32x2*)(ws + WS_XR))[xr_index((unsigned)(p * NG + grp), (unsigned)(idx - FIX))].x;
        }
      } else {
        const int i = rw * (POOL - 1) + tl;
        if (tl < POOL - 1 && i < (int)((const unsigned*)(ws + WS_SPC))[rp]) {
          const u32x2 sp = ((const u32x2*)(ws + WS_SP))[(size_t)rp * CH + i];
          if ((int)sp.y == grp) { ok = true; rec = sp.x; pb = (unsigned)rp * CH; }
        }
      }
      if (ok) {
        const unsigned dn = rec & 63u;
        const unsigned b = dn == 0 ? 0u : (dn - 1) >> 3;
        const unsigned pos = atomicAdd(&lds.tot[b], 1u);
        lds.list[b * BCAP + pos] = (rec & 0x7ffe3fu) | local_bits((unsigned)(NP * FIX + 1 + tl));
        const u32x4* row = (const u32x4*)((const char*)e_attr + rec_eoff(rec, pb));
        lds.ea[(NP * FIX + 1 + tl) * 2] = row[0];
        lds.ea[(NP * FIX + 1 + tl) * 2 + 1] = row[1];
      }
      __syncthreads();
      if (tl < BPG * 32) {
        const int b = tl >> 5, tb = (int)lds.tot[b], e = tb + (tl & 31);
        if (e < ((tb + 31) & ~31)) lds.list[b * BCAP + e] = (unsigned)((b ? 8 * b + 1 : 0) + 16) | local_bits(NP * FIX);
        if ((tl & 31) == 0) lds.tick[b] = 0u;
      }
      __syncthreads();
      allin = true;
    } else {
      wait_flag(&lds.ready, 10);
    }
    const int ntile = (__builtin_amdgcn_readfirstlane((int)lds.tot[bkt]) + 31) >> 5;
    STAMP(1, 7);
    if (first) {
      ((u32x4*)smat)[lane] = u32x4{0, 0, 0, 0};
      if (lane < 24) ((u32x4*)smat)[64 + lane] = u32x4{0, 0, 0, 0};
      const unsigned char* hb = lds.hu + (node0 + 4 * h) * RROW + (par * 64 + c) * 2;
#pragma unroll
      for (int r = 0; r < 16; ++r) {
        accA[r] = __uint_as_float((unsigned)*(const unsigned short*)(hb + crow(r & 7, 0) * RROW + (r >> 3) * 64) << 16);
        accB[r] = 0.f;
      }
      first = false;
    }

    const unsigned tka = (unsigned)(uintptr_t)(lds_ptr)&lds.tick[bkt];
#define GRAB(dst) asm volatile("ds_add_rtn_u32 %0, %1, %2" : "=v"(dst) : "v"(tka), "v"(lane == 0 ? 1u : 0u) : "memory")
    unsigned tkv;
    GRAB(tkv);
    asm volatile("s_waitcnt lgkmcnt(0)" : "+v"(tkv) :: "memory");
    const int t0 = __builtin_amdgcn_readfirstlane((int)tkv);
    if (t0 < ntile) {
      __builtin_amdgcn_s_setprio(3);
      const u32x4* l4 = (const u32x4*)&lds.list[bkt * BCAP] + h;
      const unsigned* l1 = &lds.list[bkt * BCAP + c];
      const int last = ntile - 1;
      unsigned ad[16];
#pragma unroll
      for (int qd = 0; qd < 4; ++qd) {
        const u32x4 ro = l4[8 * t0 + 2 * qd];
        ad[4 * qd + 0] = (ro.x & 0x7ffe00u) | laneoff; ad[4 * qd + 1] = (ro.y & 0x7ffe00u) | laneoff;
        ad[4 * qd + 2] = (ro.z & 0x7ffe00u) | laneoff; ad[4 * qd + 3] = (ro.w & 0x7ffe00u) | laneoff;
      }
      const unsigned e0 = l1[32 * t0];
      unsigned dn = e0 & 63u;
      f32x16 G0, G1;
      __builtin_amdgcn_sched_barrier(0);
#pragma unroll
      for (int r = 0; r < 16; ++r) G0[r] = __uint_as_float(__builtin_amdgcn_raw_buffer_load_b32(rsG, ad[r], sA, 0));
      __builtin_amdgcn_sched_barrier(0);
#pragma unroll
      for (int r = 0; r < 16; ++r) G1[r] = __uint_as_float(__builtin_amdgcn_raw_buffer_load_b32(rsG, ad[r] + 128, sA, 0));
      bf16x8 oh = *(const bf16x8*)(lutp + dn * 32);
      if (!allin) wait_rows(e0);
      bf16x8 ea = *(const bf16x8*)(eab + EA_OFF(e0));
      __builtin_amdgcn_sched_barrier(0);
      STAMP(1, 8);

#define PHASE(G, ACC, WP, AP, CB, SB, SOFF, IMM, MID)                                                              \
      {                                                                                                            \
        const bf16x8 whi = *(const bf16x8*)((WP) + (CB) * 2048);                                                   \
        const s16x4 a0 = __builtin_amdgcn_ds_read_tr16_b64_v4i16((lds_s16x4_ptr)((AP) + (CB) * 64));               \
        const s16x4 a1 = __builtin_amdgcn_ds_read_tr16_b64_v4i16((lds_s16x4_ptr)((AP) + (CB) * 64 + 8 * RROW));    \
        __builtin_amdgcn_sched_barrier(0);                                                                         \
        float ih[16];                                                                                              \
        _Pragma("unroll") for (int r = 0; r < 16; ++r) ih[r] = __uint_as_float(__float_as_uint(G[r]) << 16);      \
        G = MFMA32(ea, whi, G);                                                                                    \
        G = MFMA32(oh, __builtin_shufflevector(a0, a1, 0, 1, 2, 3, 4, 5, 6, 7), G);                                \
        asm volatile("" : "+v"(G));                                                                                \
        __builtin_amdgcn_sched_barrier(0);                                                                         \
        const bf16x8 S0 = *(const bf16x8*)(SB);                                                                    \
        const bf16x8 S1 = *(const bf16x8*)((SB) + 32);                                                             \
        MID                                                                                                        \
        __builtin_amdgcn_sched_barrier(0);                                                                         \
        unsigned P[8];                                                                                             \
        _Pragma("unroll") for (int r = 0; r < 16; r += 2) {                                                        \
          const float x0 = EXP2(G[r]), x1 = EXP2(G[r + 1]);                                                        \
          const float m0 = RCP(__builtin_fmaf(x0, ih[r], ih[r]));                                                  \
          const float m1 = RCP(__builtin_fmaf(x1, ih[r + 1], ih[r + 1]));                                          \
          P[r >> 1] = pack_bf16(m0, m1);                                                                           \
        }                                                                                                          \
        _Pragma("unroll") for (int r = 0; r < 16; ++r)                                                             \
          G[r] = __uint_as_float(__builtin_amdgcn_raw_buffer_load_b32(rsG, ad[r] + (IMM), (SOFF), 0));             \
        ACC = MFMA32(S0, __builtin_bit_cast(bf16x8, u32x4{P[0], P[1], P[2], P[3]}), ACC);                          \
        ACC = MFMA32(S1, __builtin_bit_cast(bf16x8, u32x4{P[4], P[5], P[6], P[7]}), ACC);                          \
        asm volatile("" : "+v"(ACC));                                                                              \
        __builtin_amdgcn_sched_barrier(0);                                                                         \
      }
      for (;;) {
        GRAB(tkv);
        unsigned short* sp = (unsigned short*)(sw + dn * SROW);
        *sp = (unsigned short)0x3f80;
        PHASE(G0, accA, wA, aA, 0, slo, sB, 0, )
        asm volatile("" : "+v"(tkv));
        const int nxt = __builtin_amdgcn_readfirstlane((int)tkv);
        const int nx = nxt < last ? nxt : last;
        {
          const int rem = ntile - nxt;
          if (rem == 2) __builtin_amdgcn_s_setprio(2);
          else if (rem == 1) __builtin_amdgcn_s_setprio(1);
          else if (rem <= 0) __builtin_amdgcn_s_setprio(0);
        }
        const unsigned en = l1[32 * nx];
        PHASE(G1, accA, wA, aA, 1, shi, sB, 128, )
        u32x4 ro[4];
#pragma unroll
        for (int qd = 0; qd < 4; ++qd) ro[qd] = l4[8 * nx + 2 * qd];
        PHASE(G0, accB, wB, aB, 0, slo, sA, 0,
              _Pragma("unroll") for (int qd = 0; qd < 4; ++qd) {
                ad[4 * qd + 0] = (ro[qd].x & 0x7ffe00u) | laneoff; ad[4 * qd + 1] = (ro[qd].y & 0x7ffe00u) | laneoff;
                ad[4 * qd + 2] = (ro[qd].z & 0x7ffe00u) | laneoff; ad[4 * qd + 3] = (ro[qd].w & 0x7ffe00u) | laneoff;
              }
              dn = en & 63u;)
        if (__builtin_expect(!allin, 0)) wait_rows(en);
        PHASE(G1, accB, wB, aB, 1, shi, sA, 128,
              *sp = (unsigned short)0;
              oh = *(const bf16x8*)(lutp + dn * 32);
              ea = *(const bf16x8*)(eab + EA_OFF(en));)
        if (nxt >= ntile) break;
      }
    }
    STAMP(1, 9);
    int after = -1;
    if (mode == 1 && rp + BSLOTS < NP) { rp += BSLOTS; continue; }
    if (mode == 2) {
      if ((rw + 1) * (POOL - 1) < __builtin_amdgcn_readfirstlane((int)((const unsigned*)(ws + WS_SPC))[rp])) { ++rw; continue; }
      after = rp;
    }
    int best = NP;
#pragma unroll
    for (int k = 0; k < 4; ++k) {
      unsigned long long mk = lds.omask[k];
      const int lo = (after + 1 - k + 3) >> 2;
      mk = lo >= 64 ? 0ull : (mk >> lo) << lo;
      if (mk) { const int p = 4 * (int)__builtin_ctzll(mk) + k; best = p < best ? p : best; }
    }
    best = __builtin_amdgcn_readfirstlane(best);
    if (__builtin_expect(best >= NP, 1)) break;
    mode = 2; rp = best; rw = 0;
  }
  STAMP(1, 12);
  {
    const int l2 = launder(tid) & 63;
    float* xch = (float*)lds.ea;
    WAIT_VM(0);
    lds_barrier();
    float* xo = xch + wv * 640 + l2;
#define XROWI(r) (((r) >> 3) * 5 + (((r) & 7) < 4 ? ((r) & 7) : 4))
#pragma unroll
    for (int r = 0; r < 16; ++r) if (crow(r & 7, 0) <= 8) xo[XROWI(r) * 64] = accB[r];
    lds_barrier();
    const float* xi = xch + partner * 640 + l2;
    const int nb = bkt ? 8 : 9;
    const int cc = l2 & 31, hh = l2 >> 5;
    const __amdgpu_buffer_rsrc_t rsO = __builtin_amdgcn_make_buffer_rsrc((void*)out, 0, N_NODES * 256, 0x00020000);
    const unsigned ob = (unsigned)(grp * GN + node0 + 4 * hh) * 256u + (unsigned)(par * 64 + cc) * 2u;
#pragma unroll
    for (int r = 0; r < 16; ++r) {
      const int dl0 = crow(r & 7, 0);
      if (dl0 <= 8) {
        const float z = accA[r] + xi[XROWI(r) * 64];
        const float o = z * __builtin_amdgcn_rcpf(1.f + __builtin_amdgcn_exp2f(z * NLOG2E));
        const unsigned off = ob + (unsigned)dl0 * 256u + (unsigned)(r >> 3) * 64u;
        __builtin_amdgcn_raw_buffer_store_b16((unsigned short)pack_bf16(o, 0.f), rsO, (dl0 < 8 || (hh == 0 && nb == 9)) ? off : OOB, 0, 0);
      }
    }
  }
  STAMP(1, 13);
  STAMP_FLUSH(1);
}

}

extern "C" void kernel_launch(void* const* d_in, const int* in_sizes, int n_in, void* d_out, int out_size,
                              void* d_ws, size_t ws_size, hipStream_t stream) {
  const unsigned short* x = (const unsigned short*)d_in[0];
  const unsigned short* e_attr = (const unsigned short*)d_in[1];
  const int* src = (const int*)d_in[2];
  const int* dst = (const int*)d_in[3];
  const unsigned short* u_w = (const unsigned short*)d_in[4];
  const unsigned short* v_w = (const unsigned short*)d_in[5];
  const unsigned short* g_w = (const unsigned short*)d_in[6];
  k_prep<<<SORT_BLOCKS + GEMM_BLOCKS + WPREP_BLOCKS, 512, 0, stream>>>(x, e_attr, src, dst, u_w, v_w, g_w, (char*)d_ws);
  k_edge<<<NG, NT2, 0, stream>>>(x, e_attr, (const char*)d_ws, (unsigned short*)d_out);
}
```

```cpp
#include <hip/hip_runtime.h>
#include <hip/hip_bf16.h>
#include <stdint.h>

namespace {

constexpr int N_NODES = 12500, N_EDGES = 400000, DM = 128;
constexpr int GN = 49;
constexpr int NG = 256;
constexpr int BPG = 6;
constexpr int NKEY = 12;
constexpr int SRC_HALF = 6250;
constexpr int NP = 256;
constexpr int CH = 1568;
constexpr int FIX = 8;
constexpr int SLOT_REC = 28;
constexpr int BCAP = 384;
constexpr int POOL = 192;
constexpr int BSLOTS = 6;
constexpr int NW = 12;
constexpr int NT2 = NW * 64;
constexpr float NLOG2E = -1.4426950408889634f;
constexpr unsigned OOB = 0x80000000u;

constexpr size_t TAB_BYTES = (size_t)N_NODES * 512;
constexpr size_t WS_G = 0;
constexpr size_t WS_WF = 6400000;
constexpr size_t WS_SPC = WS_WF + (576 + 4096) * 16;
constexpr size_t WS_SL = WS_SPC + 1024;
constexpr size_t WS_XR = WS_SL + (size_t)NP * NG * 64;
constexpr size_t WS_SP = WS_XR + (size_t)NP * NG * 192;
static_assert(WS_SL % 256 == 0, "alignment");

typedef short bf16x8 __attribute__((ext_vector_type(8)));
typedef short s16x4 __attribute__((ext_vector_type(4)));
typedef float f32x16 __attribute__((ext_vector_type(16)));
typedef float f32x2 __attribute__((ext_vector_type(2)));
typedef __bf16 bf16x2_t __attribute__((ext_vector_type(2)));
typedef unsigned u32x4 __attribute__((ext_vector_type(4)));
typedef unsigned u32x2 __attribute__((ext_vector_type(2)));
typedef __attribute__((address_space(3))) s16x4* lds_s16x4_ptr;
typedef __attribute__((address_space(3))) void* lds_ptr;

#define EXP2(x) __builtin_amdgcn_exp2f(x)
#define RCP(x) __builtin_amdgcn_rcpf(x)
#define MFMA32(a, b, c) __builtin_amdgcn_mfma_f32_32x32x16_bf16((a), (b), (c), 0, 0, 0)
#define DMA16R(rs, ldsa, vo, so)                                                                              \
  asm volatile("s_mov_b32 m0, %0\n\ts_nop 0\n\tbuffer_load_dwordx4 %1, %2, %3 offen lds"                   \
               :: "s"((unsigned)(ldsa)), "v"((unsigned)(vo)), "s"(rs), "s"((unsigned)(so)) : "memory")
#define WAIT_VM(n) asm volatile("s_waitcnt vmcnt(%0)" :: "n"(n) : "memory")

__device__ __forceinline__ unsigned pack_bf16(float lo, float hi) {
  f32x2 v; v.x = lo; v.y = hi;
  bf16x2_t r = __builtin_convertvector(v, bf16x2_t);
  return __builtin_bit_cast(unsigned, r);
}
__device__ __forceinline__ u32x4 make_srd(const void* base, unsigned bytes) {
  const uintptr_t a = (uintptr_t)base;
  return u32x4{(unsigned)a, (unsigned)(a >> 32) & 0xffffu, bytes, 0x00020000u};
}
__device__ __forceinline__ int crow(int reg, int h) { return (reg & 3) + 8 * (reg >> 2) + 4 * h; }
__device__ __forceinline__ int launder(int v) { asm volatile("" : "+v"(v)); return v; }
__device__ __forceinline__ void lds_barrier() { asm volatile("s_waitcnt lgkmcnt(0)\n\ts_barrier" ::: "memory"); }

__device__ __forceinline__ void wait_flag_mask(unsigned* flag, unsigned mask, unsigned n) {
  const unsigned a = (unsigned)(uintptr_t)(lds_ptr)flag;
  for (;;) {
    unsigned v;
    asm volatile("ds_read_b32 %0, %1\n\ts_waitcnt lgkmcnt(0)" : "=v"(v) : "v"(a) : "memory");
    if (((unsigned)__builtin_amdgcn_readfirstlane((int)v) & mask) >= n) break;
    __builtin_amdgcn_s_sleep(1);
  }
}
__device__ __forceinline__ void wait_flag(unsigned* flag, unsigned n) { wait_flag_mask(flag, ~0u, n); }

__device__ __forceinline__ int wave_incl_scan(int x) {
  x += __builtin_amdgcn_update_dpp(0, x, 0x111, 0xf, 0xf, false);
  x += __builtin_amdgcn_update_dpp(0, x, 0x112, 0xf, 0xf, false);
  x += __builtin_amdgcn_update_dpp(0, x, 0x114, 0xf, 0xf, false);
  x += __builtin_amdgcn_update_dpp(0, x, 0x118, 0xf, 0xf, false);
  x += __builtin_amdgcn_update_dpp(0, x, 0x142, 0xa, 0xf, false);
  x += __builtin_amdgcn_update_dpp(0, x, 0x143, 0xc, 0xf, false);
  return x;
}
__device__ __forceinline__ unsigned xr_index(unsigned s, unsigned k) { return k < 8u ? s * 8u + k : (unsigned)(NP * NG * 8) + s * 16u + (k - 8u); }
__device__ __forceinline__ unsigned local_bits(unsigned r) { return ((r & 7u) << 6) | ((r >> 3) << 23); }
__device__ __forceinline__ unsigned rec_eoff(unsigned rec, unsigned pbase) {
  return (pbase + (((rec >> 20) & 0x7f8u) | ((rec >> 6) & 7u))) * 32u;
}

constexpr int SORT_BLOCKS = NP;
constexpr int GEMM_BLOCKS = 208;
constexpr int WPREP_BLOCKS = 9;

struct SortLds {
  unsigned kc[NG * NKEY / 2];
  unsigned gc[NG];
  unsigned img[NG * 16];
  unsigned spc;
};
struct GemmLds {
  unsigned short w[2][2][128][32];
  unsigned char xt[128 * 272];
};
union PrepLds { SortLds s; GemmLds g; };
static_assert(sizeof(PrepLds) <= 81920, "two workgroups per CU");

__global__ __launch_bounds__(512, 4) void k_prep(const unsigned short* __restrict__ x, const unsigned short* __restrict__ e_attr,
                                                 const int* __restrict__ src, const int* __restrict__ dst,
                                                 const unsigned short* __restrict__ u_w, const unsigned short* __restrict__ v_w,
                                                 const unsigned short* __restrict__ g_w, char* __restrict__ ws) {
  __shared__ __attribute__((aligned(16))) PrepLds lds;
  const int tid = threadIdx.x, lane = tid & 63;
  const int wv = __builtin_amdgcn_readfirstlane(tid >> 6);
  const int blk = blockIdx.x;
  if (blk < SORT_BLOCKS) {
    const int pidx = blk;
    const int nedge = N_EDGES - pidx * CH < CH ? N_EDGES - pidx * CH : CH;
    const bool have = tid * 4 < nedge;
    int4 dv = make_int4(0, 0, 0, 0), sv = make_int4(0, 0, 0, 0);
    if (have) { dv = *(const int4*)(dst + pidx * CH + tid * 4); sv = *(const int4*)(src + pidx * CH + tid * 4); }
    if (tid < 448) ((u32x4*)lds.s.kc)[tid] = u32x4{0, 0, 0, 0};
    if (tid == 511) lds.s.spc = 0u;
    lds_barrier();
    if (have) {
      const int d4[4] = {dv.x, dv.y, dv.z, dv.w}, s4[4] = {sv.x, sv.y, sv.z, sv.w};
      unsigned g[4], dn[4], sc[4], idx[4];
#pragma unroll
      for (int k = 0; k < 4; ++k) {
        unsigned d = (unsigned)d4[k]; d = d < (unsigned)N_NODES ? d : (unsigned)(N_NODES - 1);
        sc[k] = (unsigned)s4[k]; sc[k] = sc[k] < (unsigned)N_NODES ? sc[k] : (unsigned)(N_NODES - 1);
        g[k] = d / GN; dn[k] = d - g[k] * GN;
        idx[k] = atomicAdd(&lds.s.gc[g[k]], 1u);
      }
      unsigned rk[4], k12[4];
#pragma unroll
      for (int k = 0; k < 4; ++k) {
        const unsigned b = dn[k] == 0 ? 0u : (dn[k] - 1) >> 3;
        const unsigned sh = sc[k] >= (unsigned)SRC_HALF ? 16u : 0u;
        k12[k] = b * 2 + (sh >> 4);
        rk[k] = (atomicAdd(&lds.s.kc[g[k] * 6 + b], idx[k] < (unsigned)SLOT_REC ? 1u << sh : 0u) >> sh) & 0xffffu;
      }
#pragma unroll
      for (int k = 0; k < 4; ++k) {
        const unsigned rec = (sc[k] << 9) | dn[k] | local_bits((unsigned)(tid * 4 + k));
        if (idx[k] < (unsigned)FIX) {
          lds.s.img[g[k] * 16 + 4 + idx[k]] = rec;
          ((unsigned short*)lds.s.img)[g[k] * 32 + 24 + idx[k]] = (unsigned short)(k12[k] | (rk[k] << 4));
        } else if (idx[k] < (unsigned)SLOT_REC) {
          ((u32x2*)(ws + WS_XR))[xr_index((unsigned)pidx * NG + g[k], idx[k] - FIX)] = u32x2{rec, k12[k] | (rk[k] << 4)};
        } else {
          const unsigned sp = atomicAdd(&lds.s.spc, 1u);
          ((u32x2*)(ws + WS_SP))[(size_t)pidx * CH + sp] = u32x2{rec, g[k]};
        }
      }
    }
    lds_barrier();
    if (tid < NG) {
      unsigned hd[2] = {0u, 0u};
#pragma unroll
      for (int i = 0; i < 6; ++i) {
        const unsigned cc = lds.s.kc[tid * 6 + i];
        hd[i / 3] |= ((cc & 31u) | ((cc >> 16) << 5)) << (10 * (i % 3));
      }
      ((u32x4*)lds.s.img)[tid * 4] = u32x4{hd[0], hd[1], lds.s.gc[tid], 0u};
      if (tid == 0) ((unsigned*)(ws + WS_SPC))[pidx] = lds.s.spc;
    }
    lds_barrier();
    {
      u32x4* so = (u32x4*)(ws + WS_SL) + (size_t)pidx * NG * 4;
      so[tid] = ((const u32x4*)lds.s.img)[tid];
      so[tid + 512] = ((const u32x4*)lds.s.img)[tid + 512];
    }
  } else if (blk < SORT_BLOCKS + GEMM_BLOCKS) {
    const int gb = blk - SORT_BLOCKS;
    const int j = gb >> 3;
    const int rg = (gb & 7) + 8 * (j >> 1), cp = j & 1;
    if (rg * 128 >= N_NODES) return;
    const int m = lane & 31, h = lane >> 5;
    const int cbl = wv & 1, cb = cp * 2 + cbl;
    const int rbl = wv >> 1;
    const int row0 = rg * 128 + rbl * 32;
    u32x4 wld[4], xld[4];
#pragma unroll
    for (int i = 0; i < 4; ++i) {
      const int q = tid + 512 * i;
      int xr = rg * 128 + (q >> 4); xr = xr < N_NODES ? xr : N_NODES - 1;
      xld[i] = *(const u32x4*)(x + (size_t)xr * DM + (q & 15) * 8);
    }
#pragma unroll
    for (int i = 0; i < 4; ++i) {
      const int q = tid + 512 * (i & 1);
      const unsigned short* W = (i >> 1) == 0 ? g_w + 128 * DM : v_w;
      wld[i] = *(const u32x4*)(W + (size_t)(q >> 3) * DM + cp * 64 + (q & 7) * 8);
    }
    __builtin_amdgcn_sched_barrier(0);
#pragma unroll
    for (int i = 0; i < 4; ++i) {
      const int q = tid + 512 * i;
      *(u32x4*)(lds.g.xt + (q >> 4) * 272 + (q & 15) * 16) = xld[i];
    }
#pragma unroll
    for (int i = 0; i < 4; ++i) {
      const int q = tid + 512 * (i & 1);
      *(u32x4*)&lds.g.w[i >> 1][(q >> 2) & 1][q >> 3][(q & 3) * 8] = wld[i];
    }
    lds_barrier();
    bf16x8 xa[8];
#pragma unroll
    for (int s = 0; s < 8; ++s) xa[s] = *(const bf16x8*)(lds.g.xt + (rbl * 32 + m) * 272 + s * 32 + h * 16);
    const int q4 = (lane & 15) >> 2, p4 = lane & 3, blk16 = (lane >> 4) & 1;
    const unsigned goff = (unsigned)(row0 + 4 * h) * 512u + (unsigned)(cb * 32 + m) * 4u;
    {
      f32x16 acc0, acc1;
#pragma unroll
      for (int i = 0; i < 16; ++i) { acc0[i] = 0.f; acc1[i] = 0.f; }
#pragma unroll
      for (int s = 0; s < 8; ++s) {
        const int kr = 16 * s + 8 * h + q4;
        s16x4 b0 = __builtin_amdgcn_ds_read_tr16_b64_v4i16((lds_s16x4_ptr)&lds.g.w[0][cbl][kr][blk16 * 16 + p4 * 4]);
        s16x4 b1 = __builtin_amdgcn_ds_read_tr16_b64_v4i16((lds_s16x4_ptr)&lds.g.w[0][cbl][kr + 4][blk16 * 16 + p4 * 4]);
        s16x4 v0 = __builtin_amdgcn_ds_read_tr16_b64_v4i16((lds_s16x4_ptr)&lds.g.w[1][cbl][kr][blk16 * 16 + p4 * 4]);
        s16x4 v1 = __builtin_amdgcn_ds_read_tr16_b64_v4i16((lds_s16x4_ptr)&lds.g.w[1][cbl][kr + 4][blk16 * 16 + p4 * 4]);
        acc0 = MFMA32(xa[s], __builtin_shufflevector(b0, b1, 0, 1, 2, 3, 4, 5, 6, 7), acc0);
        acc1 = MFMA32(xa[s], __builtin_shufflevector(v0, v1, 0, 1, 2, 3, 4, 5, 6, 7), acc1);
      }
      const __amdgpu_buffer_rsrc_t rsG = __builtin_amdgcn_make_buffer_rsrc((void*)(ws + WS_G), 0, (int)TAB_BYTES, 0x00020000);
#pragma unroll
      for (int r = 0; r < 16; r += 2) {
        const unsigned hvp = pack_bf16(acc1[r], acc1[r + 1]);
        float i0 = __builtin_amdgcn_rcpf(__uint_as_float(hvp << 16));
        float i1 = __builtin_amdgcn_rcpf(__uint_as_float(hvp & 0xffff0000u));
        i0 = __builtin_amdgcn_fmed3f(i0, -3.3895313892515355e38f, 3.3895313892515355e38f);
        i1 = __builtin_amdgcn_fmed3f(i1, -3.3895313892515355e38f, 3.3895313892515355e38f);
        const unsigned ip = pack_bf16(i0, i1);
#pragma unroll
        for (int k = 0; k < 2; ++k) {
          const unsigned L = k ? ip >> 16 : ip & 0xffffu;
          const float bsc = acc0[r + k] * NLOG2E;
          const unsigned mg = __float_as_uint(__builtin_amdgcn_fmed3f(__builtin_fabsf(bsc), 1.17549435e-38f, 3.37e38f));
          const unsigned t = mg + 0x8000u - L;
          const unsigned sl = (__float_as_uint(bsc) & 0x80000000u) | L;
          __builtin_amdgcn_raw_buffer_store_b32((t & 0x7fff0000u) | sl, rsG, goff + (unsigned)crow(r + k, 0) * 512u, 0, 0);
        }
      }
    }
  } else {
    u32x4* wf = (u32x4*)(ws + WS_WF);
    const int l = tid & 63;
    const int wb = blk - SORT_BLOCKS - GEMM_BLOCKS;
    if (wb > 0) {
      const int idx = (wb - 1) * 512 + tid;
      const int ks = (idx >> 6) & 7, cb = (idx >> 9) & 3;
      const unsigned short* W = (idx >> 11) ? u_w : g_w;
      unsigned v[8];
#pragma unroll
      for (int j = 0; j < 8; ++j) v[j] = W[(size_t)(16 * ks + 8 * (l >> 5) + j) * DM + cb * 32 + (l & 31)];
      wf[576 + idx] = u32x4{v[0] | (v[1] << 16), v[2] | (v[3] << 16), v[4] | (v[5] << 16), v[6] | (v[7] << 16)};
    } else if (tid < 256) {
      const int cb = tid >> 6;
      unsigned hi[4], lo[4];
#pragma unroll
      for (int jj = 0; jj < 4; ++jj) {
        const float w0 = __uint_as_float((unsigned)g_w[(size_t)(256 + 8 * (l >> 5) + 2 * jj) * DM + cb * 32 + (l & 31)] << 16) * NLOG2E;
        const float w1 = __uint_as_float((unsigned)g_w[(size_t)(256 + 8 * (l >> 5) + 2 * jj + 1) * DM + cb * 32 + (l & 31)] << 16) * NLOG2E;
        hi[jj] = pack_bf16(w0, w1);
        lo[jj] = pack_bf16(w0 - __uint_as_float(hi[jj] << 16), w1 - __uint_as_float(hi[jj] & 0xffff0000u));
      }
      wf[(cb * 2 + 0) * 64 + l] = u32x4{hi[0], hi[1], hi[2], hi[3]};
      wf[(cb * 2 + 1) * 64 + l] = u32x4{lo[0], lo[1], lo[2], lo[3]};
    } else if (tid < 320) {
      const int dl = l >> 1, hh = l & 1;
      u32x4 v = u32x4{0, 0, 0, 0};
      if (dl < 16 && ((dl >> 2) & 1) == hh) {
        const int j = ((dl >> 3) << 2) | (dl & 3);
        const unsigned one = (j & 1) ? 0x3f800000u : 0x00003f80u;
        v.x = (j >> 1) == 0 ? one : 0u; v.y = (j >> 1) == 1 ? one : 0u; v.z = (j >> 1) == 2 ? one : 0u; v.w = (j >> 1) == 3 ? one : 0u;
      }
      wf[512 + l] = v;
    }
  }
}

constexpr int SROW = 80;
constexpr int SMAT = 17 * SROW + 48;
constexpr int LISTN = BPG * BCAP;
constexpr int LDEPTH = 4;
constexpr int RROW = 272;
struct EdgeLds {
  u32x4 ea[(NP * FIX + POOL) * 2];
  u32x4 wop[8 * 64];
  u32x4 sl[NP * 4];
  unsigned char hu[58 * RROW];
  unsigned short adj[NP * NKEY];
  unsigned char ap[58 * RROW];
  u32x4 lut[64];
  unsigned list[LISTN + 32];
  unsigned char smat[NW * SMAT];
  unsigned tot[8];
  unsigned long long omask[4];
  unsigned short xdesc[256];
  unsigned tick[8];
  unsigned slow, sdone, bdone, ready, setup;
  unsigned eaprog;
};
static_assert(sizeof(EdgeLds) <= 163840, "LDS");
static_assert(NW * SMAT >= 13 * 1024, "x tile alias");
static_assert(NP * 64 + 58 * RROW + NP * NKEY * 2 >= NW * 10 * 256, "exchange alias");

__global__ __launch_bounds__(NT2) void k_edge(const unsigned short* __restrict__ x, const unsigned short* __restrict__ e_attr,
                                              const char* __restrict__ ws, unsigned short* __restrict__ out) {
  __shared__ __attribute__((aligned(16))) EdgeLds lds;
  const int tid = threadIdx.x, lane = tid & 63;
  const int wv = __builtin_amdgcn_readfirstlane(tid >> 6);
  const int grp = (blockIdx.x & 7) * 32 + (blockIdx.x >> 3);
  const int bkt = (int)((0x545432231100ull >> (4 * wv)) & 15), par = (int)((0xc56u >> wv) & 1) ;
  const int partner = (int)((0x98ba45672301ull >> (4 * wv)) & 15);
  const int c = lane & 31, h = lane >> 5;
  const u32x4 rsS = make_srd(ws + WS_SL, NP * NG * 64);
  const u32x4 rsX = make_srd(x, N_NODES * 256);
  const u32x4 rsL = make_srd(ws + WS_WF, 576 * 16);
  const __amdgpu_buffer_rsrc_t rsW = __builtin_amdgcn_make_buffer_rsrc((void*)(ws + WS_WF), 0, (576 + 4096) * 16, 0x00020000);

  const unsigned w = (unsigned)wv;
  if (wv < 8) {
    {
      const unsigned vl = (unsigned)lane * 16u;
      const unsigned vs = (unsigned)(lane >> 2) * (NG * 64) + (unsigned)(lane & 3) * 16u;
      const unsigned l_sl = (unsigned)(uintptr_t)(lds_ptr)lds.sl, l_xt = (unsigned)(uintptr_t)(lds_ptr)lds.smat;
      const unsigned xb = (unsigned)grp * (GN * 256);
      DMA16R(rsS, l_sl + w * 2048u, vs, (w * 32u * NG + (unsigned)grp) * 64u);
      DMA16R(rsS, l_sl + w * 2048u + 1024u, vs, ((w * 32u + 16u) * NG + (unsigned)grp) * 64u);
      const unsigned vx0 = (vl & 0x300u) | ((((unsigned)lane & 15u) ^ ((4u * w + ((unsigned)lane >> 4)) & 15u)) << 4);
      DMA16R(rsX, l_xt + w * 1024u, vx0, xb + w * 1024u);
      const unsigned n2 = wv < 5 ? w + 8u : w;
      DMA16R(rsX, l_xt + n2 * 1024u, vx0, xb + n2 * 1024u);
    }
  }
  if (tid == 0) { lds.sdone = 0u; lds.bdone = 0u; lds.ready = 0u; lds.eaprog = 0u; lds.setup = 0u; }
  lds_barrier();
  unsigned char* smat = lds.smat + wv * SMAT;
  if (wv == 9) {
    const unsigned l_w = (unsigned)(uintptr_t)(lds_ptr)lds.wop, l_lut = (unsigned)(uintptr_t)(lds_ptr)lds.lut;
#pragma unroll
    for (int i = 0; i < 8; ++i) DMA16R(rsL, l_w + i * 1024u, (unsigned)lane * 16u, i * 1024u);
    DMA16R(rsL, l_lut, (unsigned)lane * 16u, 512u * 16u);
    WAIT_VM(0);
    if (lane == 0) atomicAdd(&lds.ready, 1u);
  }
  const u32x4 rsEs = make_srd(e_attr, N_EDGES * 32);
  if (wv >= 9) {
    const unsigned l_ea = (unsigned)(uintptr_t)(lds_ptr)lds.ea;
    const unsigned idx = ((unsigned)lane >> 1) & 7u;
    const unsigned* sp = (const unsigned*)lds.sl + ((w - 9u) * 4u + ((unsigned)lane >> 4)) * 16u;
    unsigned pb = ((w - 9u) * 4u + ((unsigned)lane >> 4)) * CH * 32u + ((unsigned)lane & 1u) * 16u;
    wait_flag_mask(&lds.sdone, 0x10000u, 0x10000u);
    unsigned char* prog = (unsigned char*)&lds.eaprog + (w - 9u);
#pragma unroll 1
    for (int i = 0; i < 22; i += 2) {
#pragma unroll
      for (int u = 0; u < 2; ++u) {
        unsigned n = (w - 9u) + 3u * (i + u);
        const bool in = n < 64u;
        const unsigned rec = sp[in ? (i + u) * 192 + 4 + idx : 0], total = in ? sp[(i + u) * 192 + 2] : 0u;
        const unsigned vo = idx < total ? pb + (((rec >> 20) & 0x7f8u) | ((rec >> 6) & 7u)) * 32u : OOB;
        if (in) DMA16R(rsEs, l_ea + n * 1024u, vo, 0u);
        pb += 12u * CH * 32u;
      }
      if (i >= LDEPTH && i < 20) { WAIT_VM(LDEPTH); if (lane == 0) *prog = (unsigned char)(i + 2 - LDEPTH); }
    }
    WAIT_VM(0); if (lane == 0) *prog = (unsigned char)22;
    wait_flag_mask(&lds.bdone, 255u, BPG + 1);
  }
  asm volatile("" ::: "memory");
  if (wv == 8) {
    if (lane < 2) lds.ea[NP * FIX * 2 + lane] = u32x4{0, 0, 0, 0};
    wait_flag_mask(&lds.sdone, 255u, 8);
    int xn[4], sum = 0;
    bool big = false;
#pragma unroll
    for (int k = 0; k < 4; ++k) {
      const unsigned total = ((const unsigned*)lds.sl)[(lane * 4 + k) * 16 + 2];
      const unsigned tc = total < (unsigned)SLOT_REC ? total : (unsigned)SLOT_REC;
      xn[k] = tc > (unsigned)FIX ? (int)tc - FIX : 0;
      sum += xn[k];
      big |= total > (unsigned)SLOT_REC;
    }
    const int inc = wave_incl_scan(sum);
    int npool = __builtin_amdgcn_readlane(inc, 63);
    const bool slow = npool > POOL - 1;
    if (slow) npool = 0;
    if (npool > 0) {
      const int p0 = inc - sum, p1 = p0 + xn[0], p2 = p1 + xn[1], p3 = p2 + xn[2];
#pragma unroll 1
      for (int i = 0; __any(i < xn[0] || i < xn[1] || i < xn[2] || i < xn[3]); ++i) {
        const int d = (lane << 7) | i;
        if (i < xn[0]) lds.xdesc[p0 + i] = (unsigned short)d;
        if (i < xn[1]) lds.xdesc[p1 + i] = (unsigned short)(d + 32);
        if (i < xn[2]) lds.xdesc[p2 + i] = (unsigned short)(d + 64);
        if (i < xn[3]) lds.xdesc[p3 + i] = (unsigned short)(d + 96);
      }
    }
    const __amdgpu_buffer_rsrc_t rsO = __builtin_amdgcn_make_buffer_rsrc((void*)(ws + WS_XR), 0, NP * NG * 192, 0x00020000);
    u32x2 xrec[3]; unsigned xd[3];
#pragma unroll
    for (int r = 0; r < 3; ++r) {
      xd[r] = lds.xdesc[lane + 64 * r];
      const unsigned e = xr_index((xd[r] >> 5) * NG + (unsigned)grp, xd[r] & 31u);
      xrec[r] = __builtin_amdgcn_raw_buffer_load_b64(rsO, lane + 64 * r < npool ? e * 8u : OOB, 0, 0);
    }
    if (lane == 0) atomicAdd(&lds.sdone, 0x10000u);
    if (lane < 4) lds.omask[lane] = 0ull;
    if (__builtin_expect(__any(big), 0)) {
#pragma unroll
      for (int k = 0; k < 4; ++k) {
        const unsigned long long mk = __ballot(((const unsigned*)lds.sl)[(lane * 4 + k) * 16 + 2] > (unsigned)SLOT_REC);
        if (lane == 0) lds.omask[k] = mk;
      }
    }
    if (lane == 0) lds.slow = slow ? 1u : 0u;
    asm volatile("s_waitcnt lgkmcnt(0)" ::: "memory");
    if (lane == 0) atomicAdd(&lds.bdone, 1u);
    {
      const unsigned l_p = (unsigned)(uintptr_t)(lds_ptr)&lds.ea[(NP * FIX + 1) * 2];
#pragma unroll
      for (int r = 0; r < 3; ++r) {
        const unsigned eo = lane + 64 * r < npool ? rec_eoff(xrec[r].x, (xd[r] >> 5) * CH) : OOB;
#pragma unroll
        for (int q = 0; q < 2; ++q) {
          const unsigned v = (unsigned)__builtin_amdgcn_ds_bpermute((q * 32 + (lane >> 1)) * 4, (int)eo) + ((unsigned)lane & 1u) * 16u;
          if (64 * r + 32 * q < npool && (64 * r + 32 * q + 32 < POOL || lane < 62)) DMA16R(rsEs, l_p + (2 * r + q) * 1024u, v, 0u);
        }
      }
    }
    wait_flag_mask(&lds.bdone, 255u, BPG + 1);
#pragma unroll
    for (int r = 0; r < 3; ++r) {
      if (lane + 64 * r < npool) {
        const int pos = lds.adj[(xd[r] >> 5) * NKEY + (xrec[r].y & 15)] + (int)(xrec[r].y >> 4);
        if (pos < LISTN) lds.list[pos] = (xrec[r].x & 0x7ffe3fu) | local_bits((unsigned)(NP * FIX + 1 + lane + 64 * r));
      }
    }
    asm volatile("s_waitcnt lgkmcnt(0)" ::: "memory");
    if (lane == 0) atomicAdd(&lds.ready, 1u);
    WAIT_VM(0);
    if (lane == 0) ((unsigned char*)&lds.eaprog)[3] = (unsigned char)1;
  }
  asm volatile("" ::: "memory");
  if (wv < 8) {
    WAIT_VM(2);
    if (lane == 0) atomicAdd(&lds.sdone, 1u);
    bf16x8 wq[8];
#pragma unroll
    for (int ks = 0; ks < 8; ++ks)
      wq[ks] = __builtin_bit_cast(bf16x8, __builtin_amdgcn_raw_buffer_load_b128(rsW, (unsigned)lane * 16u, (576 + (wv * 8 + ks) * 64) * 16, 0));
    WAIT_VM(8);
    if (lane == 0) atomicAdd(&lds.sdone, 256u);
    wait_flag_mask(&lds.sdone, 255u, 8);
    if (wv < BPG) {
      unsigned cn[4], sum = 0;
#pragma unroll
      for (int k = 0; k < 4; ++k) {
        const unsigned f = ((const unsigned*)lds.sl)[(lane * 4 + k) * 16 + (wv >= 3)] >> (10 * (wv % 3));
        cn[k] = (f & 31u) | (((f >> 5) & 31u) << 16);
        sum += cn[k];
      }
      const unsigned inc = (unsigned)wave_incl_scan((int)sum);
      const unsigned totp = (unsigned)__builtin_amdgcn_readlane((int)inc, 63);
      unsigned run = inc - sum + ((unsigned)(wv * BCAP) | (((unsigned)(wv * BCAP) + (totp & 0xffffu)) << 16));
#pragma unroll
      for (int k = 0; k < 4; ++k) { ((unsigned*)lds.adj)[(lane * 4 + k) * 6 + wv] = run; run += cn[k]; }
      {
        const int tb = (int)((totp & 0xffffu) + (totp >> 16));
        if (lane == 0) { lds.tot[wv] = (unsigned)tb; lds.tick[wv] = 0u; }
        const int e = tb + c;
        if (e < ((tb + 31) & ~31) && e < BCAP) lds.list[wv * BCAP + e] = (unsigned)((wv ? 8 * wv + 1 : 0) + 16) | local_bits(NP * FIX);
      }
      asm volatile("s_waitcnt lgkmcnt(0)" ::: "memory");
      if (lane == 0) atomicAdd(&lds.bdone, 1u + 256u);
    }
    wait_flag(&lds.bdone, BPG * 256);
    {
      const int p = tid >> 1, j = tid & 1;
      const u32x4 rc = lds.sl[p * 4 + 1 + j];
      const u32x2 kr = ((const u32x2*)lds.sl)[p * 8 + 6 + j];
      const int total = (int)((const unsigned*)lds.sl)[p * 16 + 2] - 4 * j;
      const unsigned short* ar = &lds.adj[p * NKEY];
      int a0 = ar[kr.x & 15] + ((kr.x >> 4) & 31), a1 = ar[(kr.x >> 16) & 15] + ((kr.x >> 20) & 31);
      int a2 = ar[kr.y & 15] + ((kr.y >> 4) & 31), a3 = ar[(kr.y >> 16) & 15] + ((kr.y >> 20) & 31);
      a0 = (total > 0 && a0 < LISTN) ? a0 : LISTN + c; a1 = (total > 1 && a1 < LISTN) ? a1 : LISTN + c;
      a2 = (total > 2 && a2 < LISTN) ? a2 : LISTN + c; a3 = (total > 3 && a3 < LISTN) ? a3 : LISTN + c;
      const unsigned eb = ((unsigned)p << 23) | ((unsigned)j << 8);
      lds.list[a0] = (rc.x & 0x7ffe3fu) | eb;
      lds.list[a1] = (rc.y & 0x7ffe3fu) | (eb + 64u);
      lds.list[a2] = (rc.z & 0x7ffe3fu) | (eb + 128u);
      lds.list[a3] = (rc.w & 0x7ffe3fu) | (eb + 192u);
    }
    wait_flag_mask(&lds.sdone, 0xff00u, 8 * 256);
    const float sc = wv < 4 ? NLOG2E : 1.0f;
    unsigned char* dp = (wv < 4 ? lds.ap : lds.hu) + (4 * h) * RROW + ((wv & 3) * 32 + c) * 2;
#pragma unroll
    for (int rb = 0; rb < 2; ++rb) {
      int rr = rb * 32 + c; rr = rr < GN ? rr : GN - 1;
      const char* xa = (const char*)lds.smat + rr * 256;
      const int sw16 = (rr & 15) * 16;
      f32x16 ac;
#pragma unroll
      for (int i = 0; i < 16; ++i) ac[i] = 0.f;
#pragma unroll
      for (int ks = 0; ks < 8; ++ks) ac = MFMA32(*(const bf16x8*)(xa + ((ks * 32 + h * 16) ^ sw16)), wq[ks], ac);
#pragma unroll
      for (int r = 0; r < 16; r += 2) {
        const unsigned pk = pack_bf16(ac[r] * sc, ac[r + 1] * sc);
        if (rb * 32 + crow(r, 0) < 56 || (rb * 32 + crow(r, 0) == 56 && h == 0)) {
          *(unsigned short*)(dp + (rb * 32 + crow(r, 0)) * RROW) = (unsigned short)pk;
          *(unsigned short*)(dp + (rb * 32 + crow(r, 0) + 1) * RROW) = (unsigned short)(pk >> 16);
        }
      }
    }
    asm volatile("s_waitcnt lgkmcnt(0)" ::: "memory");
    if (lane == 0) atomicAdd(&lds.ready, 1u);
    wait_flag_mask(&lds.bdone, 255u, BPG + 1);
  }
  bool batch;
  {
    int maxtot = 0;
#pragma unroll
    for (int b = 0; b < BPG; ++b) { const int t = (int)lds.tot[b]; maxtot = t > maxtot ? t : maxtot; }
    batch = __builtin_amdgcn_readfirstlane((int)(maxtot > BCAP || lds.slow != 0u)) != 0;
  }

  const __amdgpu_buffer_rsrc_t rsG = __builtin_amdgcn_make_buffer_rsrc((void*)(ws + WS_G), 0, (int)TAB_BYTES, 0x00020000);
  const unsigned char* eab = (const unsigned char*)lds.ea + h * 16;
#define EA_OFF(e) ((((e) >> 15) & 0x1ff00u) | (((e) >> 1) & 0xe0u))
  const int node0 = bkt ? 8 * bkt + 1 : 0;
  const unsigned laneoff = (unsigned)(c * 4);
  const int sA = par * 256, sB = 256 - sA;
  const unsigned spos = (unsigned)((c >> 4) * 32 + ((c >> 2) & 1) * 16 + ((((c >> 3) & 1) << 2) | (c & 3)) * 2);
  unsigned char* sw = smat + spos - node0 * SROW;
  const unsigned char* lutp = (const unsigned char*)lds.lut + h * 16 - node0 * 32;
  const unsigned char* srow = smat + (c & 15) * SROW + h * 16;
  const unsigned char* zr = (const unsigned char*)&lds.lut[40] + h * 16;
  const unsigned char* slo = c < 16 ? srow : zr;
  const unsigned char* shi = c < 16 ? zr : srow;
  const unsigned char* wA = (const unsigned char*)lds.wop + lane * 16 + par * 4096;
  const unsigned char* wB = (const unsigned char*)lds.wop + lane * 16 + (1 - par) * 4096;
  const unsigned char* ab0 = lds.ap + (node0 + 4 * h + ((lane & 15) >> 2)) * RROW + (((lane >> 4) & 1) * 16 + (lane & 3) * 4) * 2;
  const unsigned char* aA = ab0 + par * 128;
  const unsigned char* aB = ab0 + (1 - par) * 128;
  f32x16 accA, accB;

  int mode = batch ? 1 : 0, rp = 0, rw = 0;
  bool first = true, allin = false;
  auto wait_rows = [&](unsigned e) __attribute__((always_inline)) {
    const unsigned a = (unsigned)(uintptr_t)(lds_ptr)&lds.eaprog;
    for (;;) {
      unsigned v;
      asm volatile("ds_read_b32 %0, %1\n\ts_waitcnt lgkmcnt(0)" : "=v"(v) : "v"(a) : "memory");
      const unsigned pv = (unsigned)__builtin_amdgcn_readfirstlane((int)v);
      if (pv == 0x01161616u) { allin = true; break; }
      unsigned m = pv & 255u, m1 = (pv >> 8) & 255u, m2 = (pv >> 16) & 255u;
      m = m < m1 ? m : m1; m = m < m2 ? m : m2;
      const unsigned hi = e >> 23;
      const bool pad = (e & 0xff8001c0u) == 0x80000000u;
      if (!__any(hi < 256u ? hi >= 12u * m : (!(pv >> 24) && !pad))) break;
      __builtin_amdgcn_s_sleep(2);
    }
  };
  for (;;) {
    const int tl = launder(tid);
    if (__builtin_expect(mode != 0, 0)) {
      WAIT_VM(0);
      __syncthreads();
      if (tl < 8) lds.tot[tl] = 0;
      __syncthreads();
      bool ok = false; unsigned rec = 0, pb = 0;
      if (mode == 1) {
        const int p = rp + tl / SLOT_REC, idx = tl % SLOT_REC;
        if (tl < BSLOTS * SLOT_REC && p < NP && (unsigned)idx < lds.sl[p * 4].z) {
          ok = true; pb = (unsigned)p * CH;
          rec = idx < FIX ? ((const unsigned*)lds.sl)[p * 16 + 4 + idx] : ((const u32x2*)(ws + WS_XR))[xr_index((unsigned)(p * NG + grp), (unsigned)(idx - FIX))].x;
        }
      } else {
        const int i = rw * (POOL - 1) + tl;
        if (tl < POOL - 1 && i < (int)((const unsigned*)(ws + WS_SPC))[rp]) {
          const u32x2 sp = ((const u32x2*)(ws + WS_SP))[(size_t)rp * CH + i];
          if ((int)sp.y == grp) { ok = true; rec = sp.x; pb = (unsigned)rp * CH; }
        }
      }
      if (ok) {
        const unsigned dn = rec & 63u;
        const unsigned b = dn == 0 ? 0u : (dn - 1) >> 3;
        const unsigned pos = atomicAdd(&lds.tot[b], 1u);
        lds.list[b * BCAP + pos] = (rec & 0x7ffe3fu) | local_bits((unsigned)(NP * FIX + 1 + tl));
        const u32x4* row = (const u32x4*)((const char*)e_attr + rec_eoff(rec, pb));
        lds.ea[(NP * FIX + 1 + tl) * 2] = row[0];
        lds.ea[(NP * FIX + 1 + tl) * 2 + 1] = row[1];
      }
      __syncthreads();
      if (tl < BPG * 32) {
        const int b = tl >> 5, tb = (int)lds.tot[b], e = tb + (tl & 31);
        if (e < ((tb + 31) & ~31)) lds.list[b * BCAP + e] = (unsigned)((b ? 8 * b + 1 : 0) + 16) | local_bits(NP * FIX);
        if ((tl & 31) == 0) lds.tick[b] = 0u;
      }
      __syncthreads();
      allin = true;
    } else {
      wait_flag(&lds.ready, 10);
    }
    const int ntile = (__builtin_amdgcn_readfirstlane((int)lds.tot[bkt]) + 31) >> 5;
    if (first) {
      ((u32x4*)smat)[lane] = u32x4{0, 0, 0, 0};
      if (lane < 24) ((u32x4*)smat)[64 + lane] = u32x4{0, 0, 0, 0};
      const unsigned char* hb = lds.hu + (node0 + 4 * h) * RROW + (par * 64 + c) * 2;
#pragma unroll
      for (int r = 0; r < 16; ++r) {
        accA[r] = __uint_as_float((unsigned)*(const unsigned short*)(hb + crow(r & 7, 0) * RROW + (r >> 3) * 64) << 16);
        accB[r] = 0.f;
      }
      asm volatile("s_waitcnt lgkmcnt(0)" ::: "memory");
      if (lane == 0) atomicAdd(&lds.setup, 1u);
      first = false;
    }

    const unsigned tka = (unsigned)(uintptr_t)(lds_ptr)&lds.tick[bkt];
#define GRAB(dst) asm volatile("ds_add_rtn_u32 %0, %1, %2" : "=v"(dst) : "v"(tka), "v"(lane == 0 ? 1u : 0u) : "memory")
    unsigned tkv;
    GRAB(tkv);
    asm volatile("s_waitcnt lgkmcnt(0)" : "+v"(tkv) :: "memory");
    const int t0 = __builtin_amdgcn_readfirstlane((int)tkv);
    if (t0 < ntile) {
      __builtin_amdgcn_s_setprio(3);
      const u32x4* l4 = (const u32x4*)&lds.list[bkt * BCAP] + h;
      const unsigned* l1 = &lds.list[bkt * BCAP + c];
      const int last = ntile - 1;
      unsigned ad[16];
#pragma unroll
      for (int qd = 0; qd < 4; ++qd) {
        const u32x4 ro = l4[8 * t0 + 2 * qd];
        ad[4 * qd + 0] = (ro.x & 0x7ffe00u) | laneoff; ad[4 * qd + 1] = (ro.y & 0x7ffe00u) | laneoff;
        ad[4 * qd + 2] = (ro.z & 0x7ffe00u) | laneoff; ad[4 * qd + 3] = (ro.w & 0x7ffe00u) | laneoff;
      }
      const unsigned e0 = l1[32 * t0];
      unsigned dn = e0 & 63u;
      f32x16 G0, G1;
      __builtin_amdgcn_sched_barrier(0);
#pragma unroll
      for (int r = 0; r < 16; ++r) G0[r] = __uint_as_float(__builtin_amdgcn_raw_buffer_load_b32(rsG, ad[r], sA, 0));
      __builtin_amdgcn_sched_barrier(0);
#pragma unroll
      for (int r = 0; r < 16; ++r) G1[r] = __uint_as_float(__builtin_amdgcn_raw_buffer_load_b32(rsG, ad[r] + 128, sA, 0));
      bf16x8 oh = *(const bf16x8*)(lutp + dn * 32);
      if (!allin) wait_rows(e0);
      bf16x8 ea = *(const bf16x8*)(eab + EA_OFF(e0));
      __builtin_amdgcn_sched_barrier(0);

#define PHASE(G, ACC, WP, AP, CB, SB, SOFF, IMM, MID)                                                              \
      {                                                                                                            \
        const bf16x8 whi = *(const bf16x8*)((WP) + (CB) * 2048);                                                   \
        const s16x4 a0 = __builtin_amdgcn_ds_read_tr16_b64_v4i16((lds_s16x4_ptr)((AP) + (CB) * 64));               \
        const s16x4 a1 = __builtin_amdgcn_ds_read_tr16_b64_v4i16((lds_s16x4_ptr)((AP) + (CB) * 64 + 8 * RROW));    \
        __builtin_amdgcn_sched_barrier(0);                                                                         \
        float ih[16];                                                                                              \
        _Pragma("unroll") for (int r = 0; r < 16; ++r) ih[r] = __uint_as_float(__float_as_uint(G[r]) << 16);      \
        G = MFMA32(ea, whi, G);                                                                                    \
        G = MFMA32(oh, __builtin_shufflevector(a0, a1, 0, 1, 2, 3, 4, 5, 6, 7), G);                                \
        asm volatile("" : "+v"(G));                                                                                \
        __builtin_amdgcn_sched_barrier(0);                                                                         \
        const bf16x8 S0 = *(const bf16x8*)(SB);                                                                    \
        const bf16x8 S1 = *(const bf16x8*)((SB) + 32);                                                             \
        MID                                                                                                        \
        __builtin_amdgcn_sched_barrier(0);                                                                         \
        unsigned P[8];                                                                                             \
        _Pragma("unroll") for (int r = 0; r < 16; r += 2) {                                                        \
          const float x0 = EXP2(G[r]), x1 = EXP2(G[r + 1]);                                                        \
          const float m0 = RCP(__builtin_fmaf(x0, ih[r], ih[r]));                                                  \
          const float m1 = RCP(__builtin_fmaf(x1, ih[r + 1], ih[r + 1]));                                          \
          P[r >> 1] = pack_bf16(m0, m1);                                                                           \
        }                                                                                                          \
        _Pragma("unroll") for (int r = 0; r < 16; ++r)                                                             \
          G[r] = __uint_as_float(__builtin_amdgcn_raw_buffer_load_b32(rsG, ad[r] + (IMM), (SOFF), 0));             \
        ACC = MFMA32(S0, __builtin_bit_cast(bf16x8, u32x4{P[0], P[1], P[2], P[3]}), ACC);                          \
        ACC = MFMA32(S1, __builtin_bit_cast(bf16x8, u32x4{P[4], P[5], P[6], P[7]}), ACC);                          \
        asm volatile("" : "+v"(ACC));                                                                              \
        __builtin_amdgcn_sched_barrier(0);                                                                         \
      }
      for (;;) {
        GRAB(tkv);
        unsigned short* sp = (unsigned short*)(sw + dn * SROW);
        *sp = (unsigned short)0x3f80;
        PHASE(G0, accA, wA, aA, 0, slo, sB, 0, )
        asm volatile("" : "+v"(tkv));
        const int nxt = __builtin_amdgcn_readfirstlane((int)tkv);
        const int nx = nxt < last ? nxt : last;
        {
          const int rem = ntile - nxt;
          if (rem == 2) __builtin_amdgcn_s_setprio(2);
          else if (rem == 1) __builtin_amdgcn_s_setprio(1);
          else if (rem <= 0) __builtin_amdgcn_s_setprio(0);
        }
        const unsigned en = l1[32 * nx];
        PHASE(G1, accA, wA, aA, 1, shi, sB, 128, )
        u32x4 ro[4];
#pragma unroll
        for (int qd = 0; qd < 4; ++qd) ro[qd] = l4[8 * nx + 2 * qd];
        PHASE(G0, accB, wB, aB, 0, slo, sA, 0,
              _Pragma("unroll") for (int qd = 0; qd < 4; ++qd) {
                ad[4 * qd + 0] = (ro[qd].x & 0x7ffe00u) | laneoff; ad[4 * qd + 1] = (ro[qd].y & 0x7ffe00u) | laneoff;
                ad[4 * qd + 2] = (ro[qd].z & 0x7ffe00u) | laneoff; ad[4 * qd + 3] = (ro[qd].w & 0x7ffe00u) | laneoff;
              }
              dn = en & 63u;)
        if (__builtin_expect(!allin, 0)) wait_rows(en);
        PHASE(G1, accB, wB, aB, 1, shi, sA, 128,
              *sp = (unsigned short)0;
              oh = *(const bf16x8*)(lutp + dn * 32);
              ea = *(const bf16x8*)(eab + EA_OFF(en));)
        if (nxt >= ntile) break;
      }
    }
    int after = -1;
    if (mode == 1 && rp + BSLOTS < NP) { rp += BSLOTS; continue; }
    if (mode == 2) {
      if ((rw + 1) * (POOL - 1) < __builtin_amdgcn_readfirstlane((int)((const unsigned*)(ws + WS_SPC))[rp])) { ++rw; continue; }
      after = rp;
    }
    int best = NP;
#pragma unroll
    for (int k = 0; k < 4; ++k) {
      unsigned long long mk = lds.omask[k];
      const int lo = (after + 1 - k + 3) >> 2;
      mk = lo >= 64 ? 0ull : (mk >> lo) << lo;
      if (mk) { const int p = 4 * (int)__builtin_ctzll(mk) + k; best = p < best ? p : best; }
    }
    best = __builtin_amdgcn_readfirstlane(best);
    if (__builtin_expect(best >= NP, 1)) break;
    mode = 2; rp = best; rw = 0;
  }
  {
    const int l2 = launder(tid) & 63;
    float* xch = (float*)lds.sl;
    wait_flag(&lds.setup, NW);
    float* xo = xch + wv * 640 + l2;
#define XROWI(r) (((r) >> 3) * 5 + (((r) & 7) < 4 ? ((r) & 7) : 4))
#pragma unroll
    for (int r = 0; r < 16; ++r) if (crow(r & 7, 0) <= 8) xo[XROWI(r) * 64] = accB[r];
    lds_barrier();
    const float* xi = xch + partner * 640 + l2;
    const int nb = bkt ? 8 : 9;
    const int cc = l2 & 31, hh = l2 >> 5;
    const __amdgpu_buffer_rsrc_t rsO = __builtin_amdgcn_make_buffer_rsrc((void*)out, 0, N_NODES * 256, 0x00020000);
    const unsigned ob = (unsigned)(grp * GN + node0 + 4 * hh) * 256u + (unsigned)(par * 64 + cc) * 2u;
#pragma unroll
    for (int r = 0; r < 16; ++r) {
      const int dl0 = crow(r & 7, 0);
      if (dl0 <= 8) {
        const float z = accA[r] + xi[XROWI(r) * 64];
        const float o = z * __builtin_amdgcn_rcpf(1.f + __builtin_amdgcn_exp2f(z * NLOG2E));
        const unsigned off = ob + (unsigned)dl0 * 256u + (unsigned)(r >> 3) * 64u;
        __builtin_amdgcn_raw_buffer_store_b16((unsigned short)pack_bf16(o, 0.f), rsO, (dl0 < 8 || (hh == 0 && nb == 9)) ? off : OOB, 0, 0);
      }
    }
  }
}

}

extern "C" void kernel_launch(void* const* d_in, const int* in_sizes, int n_in, void* d_out, int out_size,
                              void* d_ws, size_t ws_size, hipStream_t stream) {
  const unsigned short* x = (const unsigned short*)d_in[0];
  const unsigned short* e_attr = (const unsigned short*)d_in[1];
  const int* src = (const int*)d_in[2];
  const int* dst = (const int*)d_in[3];
  const unsigned short* u_w = (const unsigned short*)d_in[4];
  const unsigned short* v_w = (const unsigned short*)d_in[5];
  const unsigned short* g_w = (const unsigned short*)d_in[6];
  k_prep<<<SORT_BLOCKS + GEMM_BLOCKS + WPREP_BLOCKS, 512, 0, stream>>>(x, e_attr, src, dst, u_w, v_w, g_w, (char*)d_ws);
  k_edge<<<NG, NT2, 0, stream>>>(x, e_attr, (const char*)d_ws, (unsigned short*)d_out);
}
```
